```python
import torch

def _softmax(x, axis=-1):
    return torch.softmax(x, dim=axis)
def _gelu(x):
    return torch.nn.functional.gelu(x, approximate="tanh")
def _silu(x):
    return torch.nn.functional.silu(x)
def _sigmoid(x):
    return torch.sigmoid(x)
def _layernorm(x, axis=-1, eps=1e-5):
    m = x.mean(dim=axis, keepdim=True)
    v = x.var(dim=axis, keepdim=True, unbiased=False)
    return (x - m) / torch.sqrt(v + eps)
def _scatter_add(tgt, idx, src, axis=0):
    return tgt.clone().index_add_(axis, idx, src)
def _segment_sum(src, seg, n):
    out = torch.zeros((n,) + src.shape[1:], dtype=src.dtype)
    return out.index_add_(0, seg, src)
def _pad(x, pw):
    flat = tuple(v for pair in reversed(pw) for v in pair)
    return torch.nn.functional.pad(x, flat)

kernel_name = 'interleave_layers__grouped_kv_mqa__glu_gated_bilinear__prompt'

def setup_inputs():

    _w = (lambda a: (lambda t: t.to(torch.bfloat16) if t.is_floating_point() else t)(torch.as_tensor(a)))
    def _A_setup():
        B, S, D, H = 1, 2048, 512, 32
        Dh = D // H
        return {
            "x": _w((lambda *s: torch.randn(*s, dtype=torch.bfloat16) * 0.05)(B, S, D)),
            "wq": _w((lambda *s: torch.randn(*s, dtype=torch.bfloat16) * 0.05)(D, H * Dh)),
            "wk": _w((lambda *s: torch.randn(*s, dtype=torch.bfloat16) * 0.05)(D, Dh)),
            "wv": _w((lambda *s: torch.randn(*s, dtype=torch.bfloat16) * 0.05)(D, Dh)),
            "wo": _w((lambda *s: torch.randn(*s, dtype=torch.bfloat16) * 0.05)(H * Dh, D)),
        }
    _A_in = _A_setup()

    def _B_setup():
        B, S, D, F = 1, 2048, 512, 1792
        return {
            "x": _w((lambda *s: torch.randn(*s, dtype=torch.bfloat16) * 0.05)(B, S, D)),
            "w_gate": _w((lambda *s: torch.randn(*s, dtype=torch.bfloat16) * 0.4)(D, F) * (D ** -0.5)),
            "w_up": _w((lambda *s: torch.randn(*s, dtype=torch.bfloat16) * 0.4)(D, F) * (D ** -0.5)),
            "w_down": _w((lambda *s: torch.randn(*s, dtype=torch.bfloat16) * 0.4)(F, D) * (F ** -0.5)),
        }
    _B_in = _B_setup()

    return {
        "A_x": _A_in["x"],
        "A_wq": _A_in["wq"],
        "A_wk": _A_in["wk"],
        "A_wv": _A_in["wv"],
        "A_wo": _A_in["wo"],
        "B_x": _B_in["x"],
        "B_w_gate": _B_in["w_gate"],
        "B_w_up": _B_in["w_up"],
        "B_w_down": _B_in["w_down"],
    }

def reference(A_x, A_wq, A_wk, A_wv, A_wo, B_x, B_w_gate, B_w_up, B_w_down):
    def _A_ref(x, wq, wk, wv, wo):
        B, S, D = x.shape
        H = 32
        Dh = D // H
        q = (lambda x, *d: x.permute(*d))((x @ wq).reshape(B, S, H, Dh), 0, 2, 1, 3)
        k = x @ wk
        v = x @ wv
        scores = torch.einsum("bhqd,bkd->bhqk", q, k) * (Dh ** -0.5)
        p = _softmax(scores, axis=-1)
        o = torch.einsum("bhqk,bkd->bhqd", p, v)
        return (lambda x, *d: x.permute(*d))(o, 0, 2, 1, 3).reshape(B, S, H * Dh) @ wo

    def _B_ref(x, w_gate, w_up, w_down):
        g = torch.einsum("bsd,df->bsf", x, w_gate)
        u = torch.einsum("bsd,df->bsf", x, w_up)
        h = g * u
        return torch.einsum("bsf,fd->bsd", h, w_down)

    x = A_x
    x = x + _A_ref(_layernorm(x, axis=-1), A_wq, A_wk, A_wv, A_wo)
    x = x + _B_ref(_layernorm(x, axis=-1), B_w_gate, B_w_up, B_w_down)
    x = x + _A_ref(_layernorm(x, axis=-1), A_wq, A_wk, A_wv, A_wo)
    x = x + _B_ref(_layernorm(x, axis=-1), B_w_gate, B_w_up, B_w_down)
    return x
```

```cpp
#include <hip/hip_runtime.h>
#include <hip/hip_bf16.h>
#include <stdint.h>

typedef __attribute__((ext_vector_type(8))) __bf16 bf16x8;
typedef __attribute__((ext_vector_type(16))) __bf16 bf16x16;
typedef __attribute__((ext_vector_type(2))) __bf16 bf16x2;
typedef __attribute__((ext_vector_type(4))) short s16x4;
typedef __attribute__((ext_vector_type(8))) short s16x8;
typedef __attribute__((ext_vector_type(4))) float f32x4;
typedef __attribute__((ext_vector_type(8))) float f32x8;
typedef __attribute__((ext_vector_type(16))) float f32x16;
typedef __attribute__((ext_vector_type(4))) unsigned u32x4;
typedef unsigned short u16;
#define LAS __attribute__((address_space(3)))
#define DEVI __device__ __forceinline__

constexpr int S_ = 2048, D_ = 512, F_ = 1792;
constexpr float LN_EPS = 1e-5f;
constexpr float QSCALE = 0.25f * 1.4426950408889634f;
constexpr float FAST_BOUND = 60.0f;

constexpr size_t WS_WOT = 0;
constexpr size_t WS_WGUT = WS_WOT + 512 * 1024;
constexpr size_t WS_WDT = WS_WGUT + 3584 * 1024;
constexpr size_t WS_Q = WS_WDT + 1792 * 1024;
constexpr size_t WS_KV = WS_Q + 2048 * 1024;
constexpr size_t WS_KMAX = WS_KV + 136 * 1024;
constexpr size_t WS_X1 = WS_KMAX + 1024;
constexpr size_t WS_XN1 = WS_X1 + 2048 * 1024;
constexpr size_t WS_H = WS_XN1 + 2048 * 1024;
constexpr size_t WS_X2 = WS_H + 64 * 112 * 1024;
constexpr size_t WS_END = WS_X2 + 2048 * 1024;
constexpr int KVT = 2064;

DEVI int swap23(int r) { return (r & ~12) | ((r & 4) << 1) | ((r & 8) >> 1); }
DEVI float bf_lo(unsigned u) { return __builtin_bit_cast(float, u << 16); }
DEVI float bf_hi(unsigned u) { return __builtin_bit_cast(float, u & 0xffff0000u); }
DEVI unsigned pack2(float a, float b) { bf16x2 v = {(__bf16)a, (__bf16)b}; return __builtin_bit_cast(unsigned, v); }
DEVI float dot2(unsigned a, unsigned b, float c) {
  return __builtin_amdgcn_fdot2_f32_bf16(__builtin_bit_cast(bf16x2, a), __builtin_bit_cast(bf16x2, b), c, false);
}
DEVI f32x16 mfma32(u32x4 a, u32x4 b, f32x16 c) {
  return __builtin_amdgcn_mfma_f32_32x32x16_bf16(__builtin_bit_cast(bf16x8, a), __builtin_bit_cast(bf16x8, b), c, 0, 0, 0);
}
DEVI f32x4 mfma16(u32x4 a, u32x4 b, f32x4 c) {
  return __builtin_amdgcn_mfma_f32_16x16x32_bf16(__builtin_bit_cast(bf16x8, a), __builtin_bit_cast(bf16x8, b), c, 0, 0, 0);
}
DEVI f32x16 smfmac(u32x4 a, f32x8 p, f32x16 c, int idx) {
  return __builtin_amdgcn_smfmac_f32_32x32x32_bf16(__builtin_bit_cast(bf16x8, a), __builtin_bit_cast(bf16x16, p), c, idx, 0, 0);
}
DEVI float xor32(float v) { return __shfl_xor(v, 32); }
DEVI void glds16(const void* gsrc, unsigned lds_dst) {
  unsigned keep;
  asm volatile("s_mov_b32 %0, m0\n\ts_mov_b32 m0, %2\n\ts_nop 0\n\tglobal_load_lds_dwordx4 %1, off\n\ts_mov_b32 m0, %0"
               : "=&s"(keep) : "v"(gsrc), "s"(lds_dst) : "memory");
}
#define WAIT_VM(n) asm volatile("s_waitcnt vmcnt(" #n ")" ::: "memory")
#define BAR() __builtin_amdgcn_s_barrier()
DEVI void lds_barrier() { asm volatile("s_waitcnt lgkmcnt(0)" ::: "memory"); __builtin_amdgcn_s_barrier(); asm volatile("" ::: "memory"); }
DEVI u32x4 lds_rd(const char* base, int off) { return *(const LAS u32x4*)((const LAS char*)base + off); }

DEVI void prep_frag(int f, int lane, const u16* wo, const u16* wg, const u16* wu, const u16* wd, char* ws) {
  const u16* src; int ld;
  if (f < 512) {
    int nt = f >> 4, ks = f & 15;
    src = wo + (32 * ks + 8 * (lane >> 4)) * D_ + 16 * nt + (lane & 15); ld = D_;
  } else if (f < 4096) {
    int g = f - 512, nt = g >> 5, ks = g & 31, r = lane & 31;
    src = (r < 16 ? wg : wu) + (16 * ks + 8 * (lane >> 5)) * F_ + 16 * nt + swap23(r & 15); ld = F_;
  } else {
    int g = f - 4096, nt = g / 112, ks = g - nt * 112;
    src = wd + (16 * ks + 8 * (lane >> 5)) * D_ + 32 * nt + swap23(lane & 31); ld = D_;
  }
  s16x8 v;
#pragma unroll
  for (int j = 0; j < 8; j++) v[j] = (short)src[j * ld];
  *(s16x8*)(ws + (size_t)f * 1024 + lane * 16) = v;
}

template <bool PREP>
__global__ __launch_bounds__(512) void k_qkv(const u16* __restrict__ x, const u16* __restrict__ wq, const u16* __restrict__ wk,
                                             const u16* __restrict__ wv, const u16* __restrict__ wo, const u16* __restrict__ wg,
                                             const u16* __restrict__ wu, const u16* __restrict__ wd, char* __restrict__ ws) {
  extern __shared__ __attribute__((aligned(16))) char sm[];
  const int tid = threadIdx.x, lane = tid & 63;
  const int w = __builtin_amdgcn_readfirstlane(tid >> 6);
  const int b = blockIdx.x;
  const int nt = b & 7, mt = b >> 3;
  const unsigned smb = (unsigned)(size_t)(LAS char*)sm;
#pragma unroll
  for (int i = 0; i < 8; i++) {
    {
      int tok = 8 * w + (lane >> 3), c = (lane & 7) ^ ((tok >> 1) & 7);
      glds16(x + (size_t)(64 * mt + tok) * D_ + 64 * i + 8 * c, smb + i * 8192 + w * 1024);
    }
    {
      int k = 64 * i + 8 * w + (lane >> 3), c = (lane & 7) ^ (((k >> 1) & 1) << 2);
      glds16(wq + (size_t)k * D_ + 64 * nt + 8 * c, smb + 65536 + (8 * i + w) * 1024);
    }
  }
  if (nt < 2) {
#pragma unroll
    for (int i = 0; i < 2; i++) {
      glds16(wk + (2 * w + i) * 512 + lane * 8, smb + 131072 + (2 * w + i) * 1024);
      glds16(wv + (2 * w + i) * 512 + lane * 8, smb + 131072 + 16384 + (2 * w + i) * 1024);
    }
  }
  if (PREP && w >= 5) {
    int u = b * 3 + (w - 5);
#pragma unroll
    for (int i = 0; i < 8; i++) {
      int f;
      if (u < 64) f = ((u >> 3) * 4 + (i & 3)) * 16 + (u & 7) * 2 + (i >> 2);
      else if (u < 512) { int v = u - 64; f = 512 + ((v >> 4) * 4 + (i & 3)) * 32 + (v & 15) * 2 + (i >> 2); }
      else { int v = u - 512; if (v >= 224) break; int a = v / 28, c = v - a * 28; f = 4096 + (a * 2 + (i & 1)) * 112 + c * 4 + (i >> 1); }
      prep_frag(f, lane, wo, wg, wu, wd, ws);
    }
  }
  WAIT_VM(0);
  BAR();
  asm volatile("" ::: "memory");
  const bool kvw = (w == 4);
  if (w > 4 || (kvw && nt >= 2)) return;
  const int tt = kvw ? nt : (w & 1);
  const int ct = (w >> 1) & 1;
  const int hh = lane >> 5, tok = 32 * tt + (lane & 31);
  const int xaddr = tok * 128;
  const int xs = (tok >> 1) & 7;
  const int g1 = (lane >> 4) & 1, q = (lane & 15) >> 2, p = lane & 3;
  int waddr, wstep, wstep4;
  if (kvw) {
    waddr = 131072 + g1 * 16384 + (8 * hh + q) * 32 + (8 * (p & 1) + 4 * (p >> 1)) * 2; wstep = 16 * 32; wstep4 = 4 * 32;
  } else {
    int col = 32 * ct + 16 * g1 + 8 * (p & 1) + 4 * (p >> 1);
    int chunk = (col >> 3) ^ (((q >> 1) & 1) << 2);
    waddr = 65536 + (8 * hh + q) * 128 + chunk * 16 + (col & 7) * 2; wstep = 16 * 128; wstep4 = 4 * 128;
  }
  f32x16 acc = {}, cs = {};
  float sx = 0.f, sxx = 0.f;
  const u32x4 ones = {0x3f803f80u, 0x3f803f80u, 0x3f803f80u, 0x3f803f80u};
#pragma unroll 8
  for (int ks = 0; ks < 32; ks++) {
    u32x4 xf = lds_rd(sm, (ks >> 2) * 8192 + xaddr + ((((ks & 3) * 2 + hh) ^ xs) << 4));
    s16x4 a0 = __builtin_amdgcn_ds_read_tr16_b64_v4i16((LAS s16x4*)((LAS char*)sm + waddr + ks * wstep));
    s16x4 a1 = __builtin_amdgcn_ds_read_tr16_b64_v4i16((LAS s16x4*)((LAS char*)sm + waddr + ks * wstep + wstep4));
    s16x8 a8 = __builtin_shufflevector(a0, a1, 0, 1, 2, 3, 4, 5, 6, 7);
    u32x4 wf = __builtin_bit_cast(u32x4, a8);
    acc = mfma32(wf, xf, acc);
    cs = mfma32(wf, ones, cs);
#pragma unroll
    for (int j = 0; j < 4; j++) { sx = dot2(xf[j], 0x3f803f80u, sx); sxx = dot2(xf[j], xf[j], sxx); }
  }
  sx += xor32(sx); sxx += xor32(sxx);
  const float mean = sx * (1.f / D_);
  const float var = fmaxf(sxx * (1.f / D_) - mean * mean, 0.f);
  float rstd = rsqrtf(var + LN_EPS);
  const int gtok = 64 * mt + tok;
  if (!kvw) {
    rstd *= QSCALE;
    u16* qo = (u16*)(ws + WS_Q) + (size_t)gtok * D_ + 64 * nt + 32 * ct + 8 * hh;
#pragma unroll
    for (int s = 0; s < 2; s++) {
      u32x4 o;
#pragma unroll
      for (int j = 0; j < 4; j++)
        o[j] = pack2((acc[8 * s + 2 * j] - mean * cs[8 * s + 2 * j]) * rstd, (acc[8 * s + 2 * j + 1] - mean * cs[8 * s + 2 * j + 1]) * rstd);
      *(u32x4*)(qo + 16 * s) = o;
    }
  } else {
    char* img = ws + WS_KV + (size_t)(gtok >> 5) * KVT;
    u32x4 kf; float kn = 0.f;
#pragma unroll
    for (int j = 0; j < 4; j++) {
      kf[j] = pack2((acc[2 * j] - mean * cs[2 * j]) * rstd, (acc[2 * j + 1] - mean * cs[2 * j + 1]) * rstd);
      float a = bf_lo(kf[j]), c = bf_hi(kf[j]);
      kn += a * a + c * c;
    }
    *(u32x4*)(img + lane * 16) = kf;
    const int key = lane & 31;
    u16* vimg = (u16*)(img + 1024 + (key >> 4) * 512 + ((key >> 3) & 1) * 256) + (key & 7);
#pragma unroll
    for (int j = 0; j < 8; j++) {
      __bf16 v = (__bf16)((acc[8 + j] - mean * cs[8 + j]) * rstd);
      vimg[swap23(8 * hh + j) * 8] = __builtin_bit_cast(u16, v);
    }
    if (lane == 0) *(u32x4*)(img + 2048) = ones;
    kn += xor32(kn);
#pragma unroll
    for (int o = 1; o < 32; o <<= 1) kn = fmaxf(kn, __shfl_xor(kn, o));
    if (lane == 0) ((float*)(ws + WS_KMAX))[gtok >> 5] = kn;
  }
}

constexpr int ATT_O = 132096;
constexpr int ATT_Y = ATT_O + 8192;
constexpr int ATT_LDS = ATT_Y + 8 * 516 * 4;
constexpr int WO_MID = 52;
constexpr int WO_PRE = 32;

DEVI f32x8 lo8(f32x16 v) { return __builtin_shufflevector(v, v, 0, 1, 2, 3, 4, 5, 6, 7); }
DEVI f32x8 hi8(f32x16 v) { return __builtin_shufflevector(v, v, 8, 9, 10, 11, 12, 13, 14, 15); }
DEVI f32x8 exp8(f32x8 v) {
  f32x8 r;
#pragma unroll
  for (int i = 0; i < 8; i++) r[i] = __builtin_amdgcn_exp2f(v[i]);
  return r;
}

template <int T0, int T1>
DEVI void att_fast(const char* sm, int ka, int va0, int va1, u32x4 qf, f32x16& O, int idx) {
  const f32x16 z = {};
  f32x16 s0 = mfma32(lds_rd(sm, ka + T0 * KVT), qf, z);
#pragma unroll 2
  for (int t = T0; t < T1; t += 2) {
    const int tn = (t + 2 < T1) ? t + 2 : t;
    u32x4 k1 = lds_rd(sm, ka + (t + 1) * KVT);
    u32x4 k2 = lds_rd(sm, ka + tn * KVT);
    u32x4 v00 = lds_rd(sm, va0 + t * KVT), v01 = lds_rd(sm, va1 + t * KVT);
    u32x4 v10 = lds_rd(sm, va0 + (t + 1) * KVT), v11 = lds_rd(sm, va1 + (t + 1) * KVT);
    f32x16 s1 = mfma32(k1, qf, z);
    O = smfmac(v00, exp8(lo8(s0)), O, idx);
    O = smfmac(v01, exp8(hi8(s0)), O, idx);
    s0 = mfma32(k2, qf, z);
    O = smfmac(v10, exp8(lo8(s1)), O, idx);
    O = smfmac(v11, exp8(hi8(s1)), O, idx);
  }
}
template <int T0, int T1>
DEVI void att_safe(const char* sm, int ka, int va0, int va1, u32x4 qf, f32x16& O, float& m, int idx) {
  const f32x16 z = {};
#pragma unroll 1
  for (int t = T0; t < T1; t++) {
    f32x16 s = mfma32(lds_rd(sm, ka + t * KVT), qf, z);
    float mx = s[0];
#pragma unroll
    for (int i = 1; i < 16; i++) mx = fmaxf(mx, s[i]);
    mx = fmaxf(mx, xor32(mx));
    const float mn = fmaxf(m, mx);
    const float sc = __builtin_amdgcn_exp2f(m - mn);
    m = mn;
#pragma unroll
    for (int i = 0; i < 16; i++) { O[i] *= sc; s[i] -= mn; }
    O = smfmac(lds_rd(sm, va0 + t * KVT), exp8(lo8(s)), O, idx);
    O = smfmac(lds_rd(sm, va1 + t * KVT), exp8(hi8(s)), O, idx);
  }
}

__global__ __launch_bounds__(512) void k_att(const u16* xin, char* ws) {
  extern __shared__ __attribute__((aligned(16))) char sm[];
  const int tid = threadIdx.x, lane = tid & 63;
  const int w = __builtin_amdgcn_readfirstlane(tid >> 6);
  const int b = blockIdx.x;
  const int tok = 8 * b + w;
  const unsigned smb = (unsigned)(size_t)(LAS char*)sm;
  const int hh = lane >> 5, head = lane & 31;
  const u32x4 qf = *(const u32x4*)(ws + WS_Q + (size_t)tok * 1024 + head * 32 + hh * 16);
  float kmax = ((const float*)(ws + WS_KMAX))[lane];
  const char* kvg = ws + WS_KV + lane * 16;
#pragma unroll
  for (int i = 0; i < 3; i++) glds16(kvg + (8 * i + w) * 1024, smb + (8 * i + w) * 1024);
#pragma unroll
  for (int i = 3; i < 17; i++) { int pc = min(8 * i + w, 128); glds16(kvg + pc * 1024, smb + pc * 1024); }
  float qn = 0.f;
#pragma unroll
  for (int j = 0; j < 4; j++) { float a = bf_lo(qf[j]), c = bf_hi(qf[j]); qn += a * a + c * c; }
  qn += xor32(qn);
#pragma unroll
  for (int o = 1; o < 64; o <<= 1) kmax = fmaxf(kmax, __shfl_xor(kmax, o));
  const bool fast = __all(qn * kmax <= FAST_BOUND * FAST_BOUND);
  const int ka = lane * 16;
  const int va0 = (head < 16) ? 1024 + hh * 256 + head * 16 : 2048;
  const int va1 = (head < 16) ? va0 + 512 : 2048;
  int idx = 0xdddddddd;
  asm volatile("" : "+v"(idx));
  f32x16 O = {};
  float m = -3.0e38f;
  WAIT_VM(14);
  BAR();
  asm volatile("" ::: "memory");
  if (fast) att_fast<0, 8>(sm, ka, va0, va1, qf, O, idx); else att_safe<0, 8>(sm, ka, va0, va1, qf, O, m, idx);
  WAIT_VM(0);
  BAR();
  asm volatile("" ::: "memory");
  const char* wot = ws + WS_WOT + (size_t)w * 65536 + lane * 16;
  u32x4 wf[64];
#pragma unroll
  for (int i = 0; i < WO_PRE; i++) wf[i] = *(const u32x4*)(wot + ((i & 3) * 16 + (i >> 2)) * 1024);
  const u32x4 xr = *(const u32x4*)(xin + (size_t)tok * D_ + lane * 8);
  asm volatile("" ::: "memory");
  if (fast) att_fast<8, 64>(sm, ka, va0, va1, qf, O, idx); else att_safe<8, 64>(sm, ka, va0, va1, qf, O, m, idx);
  asm volatile("" ::: "memory");
#pragma unroll
  for (int i = WO_PRE; i < WO_MID; i++) wf[i] = *(const u32x4*)(wot + ((i & 3) * 16 + (i >> 2)) * 1024);
  {
    const float inv = 1.0f / O[8];
    u32x4 o;
#pragma unroll
    for (int j = 0; j < 4; j++) o[j] = pack2(O[2 * j] * inv, O[2 * j + 1] * inv);
    *(LAS u32x4*)((LAS char*)sm + ATT_O + (head * 2 + hh) * 128 + w * 16) = o;
  }
  lds_barrier();
  f32x4 y[4] = {};
#pragma unroll
  for (int ks = 0; ks < 16; ks++) {
    u32x4 of = lds_rd(sm, ATT_O + ks * 512 + (lane >> 4) * 128 + (lane & 7) * 16);
#pragma unroll
    for (int n = 0; n < 4; n++) y[n] = mfma16(wf[ks * 4 + n], of, y[n]);
    if (ks == (64 - WO_MID) / 4 - 1) {
      __builtin_amdgcn_sched_barrier(0);
#pragma unroll
      for (int i = WO_MID; i < 64; i++) wf[i] = *(const u32x4*)(wot + ((i & 3) * 16 + (i >> 2)) * 1024);
      __builtin_amdgcn_sched_barrier(0);
    }
  }
  if ((lane & 15) < 8) {
#pragma unroll
    for (int n = 0; n < 4; n++)
      *(LAS f32x4*)((LAS char*)sm + ATT_Y + ((lane & 15) * 516 + 64 * w + 16 * n + 4 * (lane >> 4)) * 4) = y[n];
  }
  lds_barrier();
  {
    const f32x4 y0 = *(const LAS f32x4*)((LAS char*)sm + ATT_Y + (w * 516 + 8 * lane) * 4);
    const f32x4 y1 = *(const LAS f32x4*)((LAS char*)sm + ATT_Y + (w * 516 + 8 * lane + 4) * 4);
    u32x4 xo;
    xo[0] = pack2(bf_lo(xr[0]) + y0[0], bf_hi(xr[0]) + y0[1]);
    xo[1] = pack2(bf_lo(xr[1]) + y0[2], bf_hi(xr[1]) + y0[3]);
    xo[2] = pack2(bf_lo(xr[2]) + y1[0], bf_hi(xr[2]) + y1[1]);
    xo[3] = pack2(bf_lo(xr[3]) + y1[2], bf_hi(xr[3]) + y1[3]);
    *(u32x4*)(ws + WS_X1 + (size_t)tok * 1024 + lane * 16) = xo;
    float v[8];
#pragma unroll
    for (int j = 0; j < 4; j++) { v[2 * j] = bf_lo(xo[j]); v[2 * j + 1] = bf_hi(xo[j]); }
    float s = 0.f;
#pragma unroll
    for (int j = 0; j < 8; j++) s += v[j];
#pragma unroll
    for (int o = 1; o < 64; o <<= 1) s += __shfl_xor(s, o);
    const float mean = s * (1.f / D_);
    float ss = 0.f;
#pragma unroll
    for (int j = 0; j < 8; j++) { v[j] -= mean; ss += v[j] * v[j]; }
#pragma unroll
    for (int o = 1; o < 64; o <<= 1) ss += __shfl_xor(ss, o);
    const float rstd = rsqrtf(ss * (1.f / D_) + LN_EPS);
    u32x4 no;
#pragma unroll
    for (int j = 0; j < 4; j++) no[j] = pack2(v[2 * j] * rstd, v[2 * j + 1] * rstd);
    *(u32x4*)(ws + WS_XN1 + (size_t)((tok >> 5) * 32 + (lane >> 1)) * 1024 + ((lane & 1) * 32 + (tok & 31)) * 16) = no;
  }
}

__global__ __launch_bounds__(512) void k_gu(char* ws) {
  extern __shared__ __attribute__((aligned(16))) char sm[];
  const int tid = threadIdx.x, lane = tid & 63;
  const int w = __builtin_amdgcn_readfirstlane(tid >> 6);
  const int b = blockIdx.x;
  const int xcd = b & 7, r = b >> 3;
  const int mt = (xcd & 1) * 8 + (r & 7), ng = (xcd >> 1) * 4 + (r >> 3);
  const unsigned smb = (unsigned)(size_t)(LAS char*)sm;
  const char* xg = ws + WS_XN1 + (size_t)mt * 131072 + lane * 16;
  const int nt = ng * 7 + (w < 7 ? w : 6);
  const char* wg = ws + WS_WGUT + (size_t)nt * 32768 + lane * 16;
  u32x4 wf[32];
#pragma unroll
  for (int c = 0; c < 4; c++) {
#pragma unroll
    for (int m = 0; m < 4; m++) glds16(xg + (m * 32 + c * 8 + w) * 1024, smb + (m * 32 + c * 8 + w) * 1024);
    if (w < 7) {
#pragma unroll
      for (int i = 0; i < 8; i++) asm volatile("global_load_dwordx4 %0, %1, off" : "=v"(wf[c * 8 + i]) : "v"(wg + (c * 8 + i) * 1024) : "memory");
    }
  }
  if (w == 7) {
    WAIT_VM(12); BAR(); WAIT_VM(8); BAR(); WAIT_VM(4); BAR(); WAIT_VM(0); BAR();
    return;
  }
  f32x16 acc[4] = {};
#define GU_STAGE(c, n)                                                                                                    \
  asm volatile("s_waitcnt vmcnt(" #n ")" : "+v"(wf[c * 8]), "+v"(wf[c * 8 + 1]), "+v"(wf[c * 8 + 2]), "+v"(wf[c * 8 + 3]), \
               "+v"(wf[c * 8 + 4]), "+v"(wf[c * 8 + 5]), "+v"(wf[c * 8 + 6]), "+v"(wf[c * 8 + 7]) :: "memory");            \
  BAR();                                                                                                                  \
  asm volatile("" ::: "memory");                                                                                         \
  _Pragma("unroll") for (int i = 0; i < 8; i++) {                                                                         \
    _Pragma("unroll") for (int m = 0; m < 4; m++)                                                                         \
      acc[m] = mfma32(wf[c * 8 + i], lds_rd(sm, (m * 32 + c * 8 + i) * 1024 + lane * 16), acc[m]);                        \
  }
  GU_STAGE(0, 36)
  GU_STAGE(1, 24)
  GU_STAGE(2, 12)
  GU_STAGE(3, 0)
#undef GU_STAGE
#pragma unroll
  for (int m = 0; m < 4; m++) {
    u32x4 o;
#pragma unroll
    for (int j = 0; j < 4; j++) o[j] = pack2(acc[m][2 * j] * acc[m][8 + 2 * j], acc[m][2 * j + 1] * acc[m][8 + 2 * j + 1]);
    *(u32x4*)(ws + WS_H + (size_t)((mt * 4 + m) * 112 + nt) * 1024 + lane * 16) = o;
  }
}

constexpr int DN_PF = 10;
__global__ __launch_bounds__(512) void k_dn(char* ws, u16* out) {
  extern __shared__ __attribute__((aligned(16))) char sm[];
  const int tid = threadIdx.x, lane = tid & 63;
  const int w = __builtin_amdgcn_readfirstlane(tid >> 6);
  const int b = blockIdx.x;
  const int xcd = b & 7, r = b >> 3;
  const int mt = (xcd >> 1) * 8 + (r & 7), nt = (xcd & 1) * 4 + (r >> 3);
  const char* hg = ws + WS_H + (size_t)(mt * 2 * 112 + w * 14) * 1024 + lane * 16;
  const char* wg = ws + WS_WDT + (size_t)(nt * 2 * 112 + w * 14) * 1024 + lane * 16;
  u32x4 hf[14][2], wf[14][2];
#define DN_LOAD(i)                                                                               \
  wf[i][0] = *(const u32x4*)(wg + (i) * 1024); wf[i][1] = *(const u32x4*)(wg + (112 + (i)) * 1024); \
  hf[i][0] = *(const u32x4*)(hg + (i) * 1024); hf[i][1] = *(const u32x4*)(hg + (112 + (i)) * 1024);
#pragma unroll
  for (int i = 0; i < DN_PF; i++) { DN_LOAD(i) }
  const int rt = tid >> 3, rc = tid & 7;
  const u32x4 xr = *(const u32x4*)(ws + WS_X1 + (size_t)(64 * mt + rt) * 1024 + (64 * nt + 8 * rc) * 2);
  f32x16 acc[2][2] = {};
#pragma unroll
  for (int i = 0; i < 14; i++) {
    __builtin_amdgcn_sched_barrier(0);
#pragma unroll
    for (int n = 0; n < 2; n++)
#pragma unroll
      for (int m = 0; m < 2; m++) acc[n][m] = mfma32(wf[i][n], hf[i][m], acc[n][m]);
    __builtin_amdgcn_sched_barrier(0);
    if (i + DN_PF < 14) { DN_LOAD(i + DN_PF) }
  }
#undef DN_LOAD
  const int hh = lane >> 5;
#pragma unroll
  for (int n = 0; n < 2; n++)
#pragma unroll
    for (int m = 0; m < 2; m++)
#pragma unroll
      for (int g = 0; g < 4; g++) {
        f32x4 v = {acc[n][m][4 * g], acc[n][m][4 * g + 1], acc[n][m][4 * g + 2], acc[n][m][4 * g + 3]};
        *(LAS f32x4*)((LAS char*)sm + (w * 64 + m * 32 + (lane & 31)) * 272 + (32 * n + 16 * (g >> 1) + 8 * hh + 4 * (g & 1)) * 4) = v;
      }
  lds_barrier();
  f32x4 s0 = {}, s1 = {};
#pragma unroll
  for (int i = 0; i < 8; i++) {
    s0 += *(const LAS f32x4*)((LAS char*)sm + (i * 64 + rt) * 272 + rc * 32);
    s1 += *(const LAS f32x4*)((LAS char*)sm + (i * 64 + rt) * 272 + rc * 32 + 16);
  }
  u32x4 o;
  o[0] = pack2(bf_lo(xr[0]) + s0[0], bf_hi(xr[0]) + s0[1]);
  o[1] = pack2(bf_lo(xr[1]) + s0[2], bf_hi(xr[1]) + s0[3]);
  o[2] = pack2(bf_lo(xr[2]) + s1[0], bf_hi(xr[2]) + s1[1]);
  o[3] = pack2(bf_lo(xr[3]) + s1[2], bf_hi(xr[3]) + s1[3]);
  *(u32x4*)(out + (size_t)(64 * mt + rt) * D_ + 64 * nt + 8 * rc) = o;
}

extern "C" void kernel_launch(void* const* d_in, const int* in_sizes, int n_in, void* d_out, int out_size, void* d_ws,
                              size_t ws_size, hipStream_t stream) {
  const u16* x = (const u16*)d_in[0];
  const u16* wq = (const u16*)d_in[1];
  const u16* wk = (const u16*)d_in[2];
  const u16* wv = (const u16*)d_in[3];
  const u16* wo = (const u16*)d_in[4];
  const u16* wg = (const u16*)d_in[6];
  const u16* wu = (const u16*)d_in[7];
  const u16* wd = (const u16*)d_in[8];
  char* ws = (char*)d_ws;
  u16* x2 = (u16*)(ws + WS_X2);
  (void)hipFuncSetAttribute((const void*)k_qkv<true>, hipFuncAttributeMaxDynamicSharedMemorySize, 163840);
  (void)hipFuncSetAttribute((const void*)k_qkv<false>, hipFuncAttributeMaxDynamicSharedMemorySize, 163840);
  (void)hipFuncSetAttribute((const void*)k_att, hipFuncAttributeMaxDynamicSharedMemorySize, ATT_LDS);
  (void)hipFuncSetAttribute((const void*)k_gu, hipFuncAttributeMaxDynamicSharedMemorySize, 131072);
  (void)hipFuncSetAttribute((const void*)k_dn, hipFuncAttributeMaxDynamicSharedMemorySize, 8 * 64 * 272);
  k_qkv<true><<<256, 512, 163840, stream>>>(x, wq, wk, wv, wo, wg, wu, wd, ws);
  k_att<<<256, 512, ATT_LDS, stream>>>(x, ws);
  k_gu<<<256, 512, 131072, stream>>>(ws);
  k_dn<<<256, 512, 8 * 64 * 272, stream>>>(ws, x2);
  k_qkv<false><<<256, 512, 163840, stream>>>(x2, wq, wk, wv, wo, wg, wu, wd, ws);
  k_att<<<256, 512, ATT_LDS, stream>>>(x2, ws);
  k_gu<<<256, 512, 131072, stream>>>(ws);
  k_dn<<<256, 512, 8 * 64 * 272, stream>>>(ws, (u16*)d_out);
}
```

```cpp
#include <hip/hip_runtime.h>
#include <hip/hip_bf16.h>
#include <stdint.h>

typedef __attribute__((ext_vector_type(8))) __bf16 bf16x8;
typedef __attribute__((ext_vector_type(16))) __bf16 bf16x16;
typedef __attribute__((ext_vector_type(2))) __bf16 bf16x2;
typedef __attribute__((ext_vector_type(4))) short s16x4;
typedef __attribute__((ext_vector_type(8))) short s16x8;
typedef __attribute__((ext_vector_type(2))) float f32x2;
typedef __attribute__((ext_vector_type(4))) float f32x4;
typedef __attribute__((ext_vector_type(8))) float f32x8;
typedef __attribute__((ext_vector_type(16))) float f32x16;
typedef __attribute__((ext_vector_type(2))) unsigned u32x2;
typedef __attribute__((ext_vector_type(4))) unsigned u32x4;
typedef __attribute__((ext_vector_type(8))) unsigned u32x8;
typedef __attribute__((ext_vector_type(16))) unsigned u32x16;
typedef __attribute__((ext_vector_type(32))) unsigned u32x32;
typedef unsigned short u16;
typedef unsigned long long u64;
typedef __amdgpu_buffer_rsrc_t rsrc_t;
#define LAS __attribute__((address_space(3)))
#define GAS __attribute__((address_space(1)))
#define DEVI __device__ __forceinline__

constexpr int D_ = 512, F_ = 1792;
constexpr float LN_EPS = 1e-5f;
constexpr float QSCALE = 0.25f * 1.4426950408889634f;
constexpr float FAST_BOUND = 60.0f;
constexpr int KVT = 2112;
constexpr int KVH = 8704, KVG = 2 * KVH;

constexpr unsigned WS_EPOCH = 0;
constexpr unsigned FL = 128;
constexpr unsigned WS_FKV = 4096;
constexpr unsigned WS_FPREP = WS_FKV + 2 * 8 * 256 * FL;
constexpr unsigned WS_KV = WS_FPREP + 8 * 256 * FL;
constexpr unsigned KV_MAX = 136 * 1024, KV_SIZE = KV_MAX + 1024;
constexpr unsigned WS_WOT = 0x180000;
constexpr unsigned WS_WGUT = WS_WOT + 512 * 1024;
constexpr unsigned WS_WDT = WS_WGUT + 3584 * 1024;
constexpr unsigned WS_GRP = 0x800000, G_SIZE = 0x200000;
constexpr unsigned G_O = 0;
constexpr unsigned G_X1 = G_O + 256 * 1024;
constexpr unsigned G_X2 = G_X1 + 256 * 1024;
constexpr unsigned G_H = G_X2 + 256 * 1024;
constexpr unsigned G_ST = G_H + 8 * 112 * 1024;
constexpr unsigned G_FO = G_ST + 32 * 2048;
constexpr unsigned G_FX = G_FO + 32 * 8 * FL;
constexpr unsigned G_FH = G_FX + 32 * 8 * FL;
constexpr unsigned G_FDN = G_FH + 64 * FL;
constexpr unsigned G_END = G_FDN + 32 * 8 * FL;
constexpr unsigned WS_END = WS_GRP + 8 * G_SIZE;
static_assert(WS_KV + 2 * KV_SIZE <= WS_WOT && WS_WDT + 1800 * 1024 <= WS_GRP && G_END <= G_SIZE, "workspace map");
#define B_NAP 12
#define BACK 11
#define P_AT 2
#define W_NAP 4
#define WO_PRE 10
#define D_NAP 12
#define K_NAP 14
#define O_NAP 16
#define X_NAP 12
#define H_NAP 20
#define TOUCH_M 63
constexpr int LDS_BYTES = 163840;

#define STAMP(i)

DEVI int swap23(int r) { return (r & ~12) | ((r & 4) << 1) | ((r & 8) >> 1); }
DEVI float bf_lo(unsigned u) { return __builtin_bit_cast(float, u << 16); }
DEVI float bf_hi(unsigned u) { return __builtin_bit_cast(float, u & 0xffff0000u); }
DEVI unsigned pack2(float a, float b) { bf16x2 v = {(__bf16)a, (__bf16)b}; return __builtin_bit_cast(unsigned, v); }
DEVI float dot2(unsigned a, unsigned b, float c) {
  return __builtin_amdgcn_fdot2_f32_bf16(__builtin_bit_cast(bf16x2, a), __builtin_bit_cast(bf16x2, b), c, false);
}
DEVI f32x16 mfma32(u32x4 a, u32x4 b, f32x16 c) {
  return __builtin_amdgcn_mfma_f32_32x32x16_bf16(__builtin_bit_cast(bf16x8, a), __builtin_bit_cast(bf16x8, b), c, 0, 0, 0);
}
DEVI f32x4 mfma16(u32x4 a, u32x4 b, f32x4 c) {
  return __builtin_amdgcn_mfma_f32_16x16x32_bf16(__builtin_bit_cast(bf16x8, a), __builtin_bit_cast(bf16x8, b), c, 0, 0, 0);
}
DEVI f32x16 smfmac(u32x4 a, f32x8 p, f32x16 c, int idx) {
  return __builtin_amdgcn_smfmac_f32_32x32x32_bf16(__builtin_bit_cast(bf16x8, a), __builtin_bit_cast(bf16x16, p), c, idx, 0, 0);
}
DEVI float add1(float a, float b) { float r; asm("v_add_f32 %0, %1, %2" : "=v"(r) : "v"(a), "v"(b)); return r; }
DEVI float fma1(float a, float b, float c) { float r; asm("v_fma_f32 %0, %1, %2, %3" : "=v"(r) : "v"(a), "v"(b), "v"(c)); return r; }
DEVI float mul1(float a, float b) { float r; asm("v_mul_f32 %0, %1, %2" : "=v"(r) : "v"(a), "v"(b)); return r; }
template <int CTRL> DEVI float dpp(float v) {
  return __builtin_bit_cast(float, __builtin_amdgcn_update_dpp(0, __builtin_bit_cast(int, v), CTRL, 0xf, 0xf, true));
}
struct OpAdd { static DEVI float f(float a, float b) { return a + b; } };
struct OpMax { static DEVI float f(float a, float b) { return fmaxf(a, b); } };
template <class Op> DEVI float red32(float v) {
  unsigned u = __builtin_bit_cast(unsigned, v);
  auto r = __builtin_amdgcn_permlane32_swap(u, u, false, false);
  return Op::f(__builtin_bit_cast(float, (unsigned)r[0]), __builtin_bit_cast(float, (unsigned)r[1]));
}
template <class Op> DEVI float red16(float v) {
  unsigned u = __builtin_bit_cast(unsigned, v);
  auto r = __builtin_amdgcn_permlane16_swap(u, u, false, false);
  return Op::f(__builtin_bit_cast(float, (unsigned)r[0]), __builtin_bit_cast(float, (unsigned)r[1]));
}
template <class Op> DEVI float red8(float v) { return Op::f(v, dpp<0x128>(v)); }
template <class Op> DEVI float red_row(float v) {
  v = Op::f(v, dpp<0xB1>(v)); v = Op::f(v, dpp<0x4E>(v)); v = Op::f(v, dpp<0x141>(v)); v = Op::f(v, dpp<0x140>(v));
  return v;
}
template <class Op> DEVI float red_wave(float v) { return red32<Op>(red16<Op>(red_row<Op>(v))); }

DEVI void glds(unsigned voff, const void* sbase, unsigned lds_dst) {
  unsigned keep;
  asm volatile("s_mov_b32 %0, m0\n\ts_mov_b32 m0, %3\n\ts_nop 0\n\tglobal_load_lds_dwordx4 %1, %2 sc1\n\ts_mov_b32 m0, %0"
               : "=&s"(keep) : "v"(voff), "s"(sbase), "s"(lds_dst) : "memory");
}
DEVI void glds4(unsigned voff, const void* sbase, unsigned lds_dst) {
  unsigned keep;
  asm volatile("s_mov_b32 %0, m0\n\ts_mov_b32 m0, %3\n\ts_nop 0\n\tglobal_load_lds_dwordx4 %1, %2 sc1\n\t"
               "global_load_lds_dwordx4 %1, %2 offset:1024 sc1\n\tglobal_load_lds_dwordx4 %1, %2 offset:2048 sc1\n\t"
               "global_load_lds_dwordx4 %1, %2 offset:3072 sc1\n\ts_mov_b32 m0, %0"
               : "=&s"(keep) : "v"(voff), "s"(sbase), "s"(lds_dst) : "memory");
}
#define WAIT_VM(n) asm volatile("s_waitcnt vmcnt(" #n ")" ::: "memory")
DEVI void bar() { asm volatile("" ::: "memory"); __builtin_amdgcn_s_barrier(); asm volatile("" ::: "memory"); }
DEVI void lds_bar() { asm volatile("s_waitcnt lgkmcnt(0)" ::: "memory"); __builtin_amdgcn_s_barrier(); asm volatile("" ::: "memory"); }
#define SCHED() __builtin_amdgcn_sched_barrier(0)
DEVI u32x4 lds_rd(LAS char* sm, int off) { return *(const LAS u32x4*)(sm + off); }
DEVI f32x4 lds_rdf(LAS char* sm, int off) { return *(const LAS f32x4*)(sm + off); }
DEVI u32x4 lds_tr(LAS char* sm, int off, int off2) {
  s16x4 a0 = __builtin_amdgcn_ds_read_tr16_b64_v4i16((LAS s16x4*)(sm + off));
  s16x4 a1 = __builtin_amdgcn_ds_read_tr16_b64_v4i16((LAS s16x4*)(sm + off2));
  return __builtin_bit_cast(u32x4, __builtin_shufflevector(a0, a1, 0, 1, 2, 3, 4, 5, 6, 7));
}
DEVI u32x4 ld16(rsrc_t r, unsigned voff, unsigned soff) { return __builtin_amdgcn_raw_buffer_load_b128(r, voff, soff, 16); }
DEVI void st16(rsrc_t r, unsigned voff, unsigned soff, u32x4 v) { __builtin_amdgcn_raw_buffer_store_b128(v, r, voff + soff, 0, 16); }

DEVI int lane_id() { unsigned m = ~0u; asm volatile("" : "+s"(m)); return __builtin_amdgcn_mbcnt_hi(m, __builtin_amdgcn_mbcnt_lo(m, 0)) & 63; }
DEVI unsigned fresh(unsigned x) { asm volatile("" : "+s"(x)); return x; }
DEVI u64 opaque_t(u64 x) { asm volatile("" : "+s"(x)); return x; }
DEVI int opaque_s(int x) { asm volatile("" : "+s"(x)); return x; }
#define PHASE_IDS()                                                                                   \
  const int w = opaque_s(w_), g = opaque_s(g_), r = opaque_s(r_), vb = g * 32 + r;                    \
  const int lane = lane_id(), tid = w * 64 + lane;                                                    \
  const unsigned smb = opaque_s((unsigned)(size_t)sm);                                                \
  const int m4 = r >> 3, m64 = g * 4 + m4, nt8 = r & 7;                                               \
  const unsigned gb = WS_GRP + g * G_SIZE;                                                            \
  const unsigned l16 = lane * 16;                                                                     \
  (void)gb; (void)smb; (void)tid; (void)vb; (void)m64; (void)nt8; (void)l16;
constexpr int SPIN_CAP = 1 << 18;
DEVI void poll(const char* base, unsigned voff, u64 tag) {
#pragma unroll 1
  for (int s = 0; s < SPIN_CAP; s++) {
    u32x2 v;
    asm volatile("global_load_dwordx2 %0, %1, %2 sc1\n\ts_waitcnt vmcnt(0)" : "=&v"(v) : "v"(voff), "s"(base) : "memory");
    if (__all(((v[0] ^ (unsigned)tag) | (v[1] ^ (unsigned)(tag >> 32))) == 0)) break;
    __builtin_amdgcn_s_sleep(1);
  }
}
template <int NAP = 2>
DEVI void lds_wait(LAS char* p, int val) {
  while (__builtin_amdgcn_readfirstlane(__hip_atomic_load((LAS int*)p, __ATOMIC_RELAXED, __HIP_MEMORY_SCOPE_WORKGROUP)) < val)
    __builtin_amdgcn_s_sleep(NAP);
  asm volatile("" ::: "memory");
}
DEVI int lds_add(LAS char* p) {
  int o = 0;
  asm volatile("s_waitcnt lgkmcnt(0)" ::: "memory");
  if (lane_id() == 0) o = __hip_atomic_fetch_add((LAS int*)p, 1, __ATOMIC_RELAXED, __HIP_MEMORY_SCOPE_WORKGROUP);
  return __builtin_amdgcn_readfirstlane(o);
}
DEVI void lds_inc(LAS char* p) {
  asm volatile("s_mov_b64 exec, 1\n\tds_add_u32 %0, %1\n\ts_mov_b64 exec, -1" :: "v"(fresh((unsigned)(size_t)p)), "v"(fresh(1)) : "memory");
}
DEVI void lds_post(LAS char* p, int val) { __hip_atomic_store((LAS int*)p, val, __ATOMIC_RELAXED, __HIP_MEMORY_SCOPE_WORKGROUP); }
DEVI void set_flag1(const char* p, u64 tag) {
  asm volatile("s_mov_b64 exec, 1\n\tglobal_store_dwordx2 %0, %1, %2 sc1\n\ts_mov_b64 exec, -1" :: "v"(fresh(0)), "v"(tag), "s"(p) : "memory");
}
DEVI void set_flag(char* ws, unsigned off, u64 tag) {
  asm volatile("" : "+s"(tag));
  __hip_atomic_store((GAS u64*)(ws + off), tag, __ATOMIC_RELAXED, __HIP_MEMORY_SCOPE_AGENT);
}

DEVI f32x8 lo8(f32x16 v) { return __builtin_shufflevector(v, v, 0, 1, 2, 3, 4, 5, 6, 7); }
DEVI f32x8 hi8(f32x16 v) { return __builtin_shufflevector(v, v, 8, 9, 10, 11, 12, 13, 14, 15); }
DEVI f32x8 exp8(f32x8 v) {
  f32x8 r;
#pragma unroll
  for (int i = 0; i < 8; i++) r[i] = __builtin_amdgcn_exp2f(v[i]);
  return r;
}
DEVI void att_safe(LAS char* sm, int ka, int va, u32x4 qf, f32x16& O, float& m, int idx) {
  const f32x16 z = {};
  m = -3.0e38f;
  float zero = 0.f;
  asm volatile("" : "+v"(zero));
#pragma unroll
  for (int i = 0; i < 16; i++) O[i] = zero;
#pragma unroll 1
  for (int t = 0; t < 32; t++) {
    const int to = (t >> 2) * KVH + (t & 3) * KVT;
    f32x16 s = mfma32(lds_rd(sm, ka + to), qf, z);
    float mx = s[0];
#pragma unroll
    for (int i = 1; i < 16; i++) mx = fmaxf(mx, s[i]);
    mx = red32<OpMax>(mx);
    const float mn = fmaxf(m, mx);
    const float sc = __builtin_amdgcn_exp2f(m - mn);
    m = mn;
#pragma unroll
    for (int i = 0; i < 16; i++) { O[i] *= sc; s[i] -= mn; }
    O = smfmac(lds_rd(sm, va + to), exp8(lo8(s)), O, idx);
    O = smfmac(lds_rd(sm, va + 544 + to), exp8(hi8(s)), O, idx);
  }
}
#define STR2(x) #x
#define STR(x) STR2(x)
#define EX(r) "v_exp_f32 v" #r ", v" #r "\n\t"
#define SPV(o, a, b) "v_smfmac_f32_32x32x32_bf16 %[" o "], v[" a "], v[" b "], %[idx]\n\t"
#define MQK(d, q) "v_mfma_f32_32x32x16_bf16 v[" d "], v[244:247], %[" q "], 0\n\t"
#define RDK(t) "ds_read_b128 v[244:247], %[ka] offset:" STR((t) * 2112 + ((t) / 4) * 256) "\n\t"
#define RDV0(t) "ds_read_b128 v[248:251], %[va] offset:" STR((t) * 2112 + ((t) / 4) * 256) "\n\t"
#define RDV1(t) "ds_read_b128 v[252:255], %[va] offset:" STR((t) * 2112 + ((t) / 4) * 256 + 544) "\n\t"
#define WL(n) "s_waitcnt lgkmcnt(" #n ")\n\t"
#define ATT_TILE(t, X, Y)                                                                            \
  EX(212) EX(213) SPV("Ob", "252:255", "236:243") RDV1(t) EX(214) EX(215) EX(216) EX(217)              \
  MQK("228:243", "qb") RDK(t + 1) EX(218) EX(219) EX(220) EX(221) EX(222) EX(223)                      \
  WL(2) SPV("Oa", "248:251", "212:219") X EX(224) EX(225) EX(226) EX(227)                              \
  EX(228) EX(229) WL(0) SPV("Oa", "252:255", "220:227") EX(230) EX(231) EX(232) EX(233)                \
  MQK("212:227", "qa") Y EX(234) EX(235) EX(236) EX(237) EX(238) EX(239)                               \
  SPV("Ob", "248:251", "228:235") RDV0(t + 1) EX(240) EX(241) EX(242) EX(243)
#define ATT_HEAD                                                                                     \
  "s_waitcnt lgkmcnt(0)\n\t"                                                                         \
  RDK(0) RDV0(0) "s_mov_b32 s45, m0\n\t"                                                             \
  "v_mov_b32 v252, 0\n\tv_mov_b32 v253, 0\n\tv_mov_b32 v254, 0\n\tv_mov_b32 v255, 0\n\t"             \
  "v_mov_b32 v236, 0\n\tv_mov_b32 v237, 0\n\tv_mov_b32 v238, 0\n\tv_mov_b32 v239, 0\n\t"             \
  "v_mov_b32 v240, 0\n\tv_mov_b32 v241, 0\n\tv_mov_b32 v242, 0\n\tv_mov_b32 v243, 0\n\t"             \
  "v_mfma_f32_32x32x16_bf16 %[Oa], v[252:255], v[252:255], 0\n\t"                                    \
  "v_mfma_f32_32x32x16_bf16 %[Ob], v[252:255], v[252:255], 0\n\t"                                    \
  "s_waitcnt lgkmcnt(0)\n\t"                                                                         \
  MQK("212:227", "qa")                                                                               \
  "s_nop 7\n\ts_nop 7\n\t"
#define ATT_NEXT "v_add_u32 %[ka], 0x8800, %[ka]\n\tv_add_u32 %[va], 0x8800, %[va]\n\t"
#define ATT_TAIL                                                                                     \
  "s_mov_b32 m0, s45\n\ts_waitcnt vmcnt(0) lgkmcnt(0)\n\t"                                           \
  SPV("Ob", "252:255", "236:243")                                                                    \
  "s_nop 7\n\ts_nop 7\n\t"
#define ATT_CLOB "scc", "memory",                                                                                                 \
  "v212", "v213", "v214", "v215", "v216", "v217", "v218", "v219", "v220", "v221", "v222", "v223", "v224", "v225", "v226", "v227", \
  "v228", "v229", "v230", "v231", "v232", "v233", "v234", "v235", "v236", "v237", "v238", "v239", "v240", "v241", "v242", "v243", \
  "v244", "v245", "v246", "v247", "v248", "v249", "v250", "v251", "v252", "v253", "v254", "v255"
#define WLD(r, o) "global_load_dwordx4 v[" STR(r) ":" STR(r + 3) "], %[vo], s[42:43] offset:" STR(o) " sc1\n\t"
#define WLD8(r) WLD(r, -4096) WLD(r + 4, -3072) WLD(r + 8, -2048) WLD(r + 12, -1024) WLD(r + 16, 0) WLD(r + 20, 1024) WLD(r + 24, 2048) WLD(r + 28, 3072) \
  "s_add_u32 s42, s42, 0x2000\n\ts_addc_u32 s43, s43, 0\n\t"
#define WLD4A(r) WLD(r, -4096) WLD(r + 4, -3072) WLD(r + 8, -2048) WLD(r + 12, -1024)
#define WLD4B(r) WLD(r, 0) WLD(r + 4, 1024) WLD(r + 8, 2048) WLD(r + 12, 3072) "s_add_u32 s42, s42, 0x2000\n\ts_addc_u32 s43, s43, 0\n\t"
#define WDMA(o) "global_load_lds_dwordx4 %[vo], s[42:43] offset:" #o " sc1\n\t"
#define BUMP(n) "s_add_u32 s42, s42, " #n "\n\ts_addc_u32 s43, s43, 0\n\t"
#define PRIO(n) "s_setprio " #n "\n\t"
#define ATT_WAIT(n, o, sh)                                                                                               \
  "Lw" #n "%=:\n\tds_read_b32 v52, %[sy] offset:" #o "\n\t" WL(0) "v_readfirstlane_b32 s44, v52\n\ts_nop 3\n\ts_lshr_b32 s44, s44, " #sh "\n\t" \
  "s_cmp_ge_i32 s44, %[need]\n\ts_cbranch_scc1 Lg" #n "%=\n\ts_sleep 1\n\ts_branch Lw" #n "%=\n\tLg" #n "%=:\n\t"
#define ATT_POST(o) "s_mov_b64 exec, 1\n\tds_inc_u32 %[sy], %[idx] offset:" #o "\n\ts_mov_b64 exec, -1\n\t" WL(0)
#define ATT_TILED                                                                                                        \
  "s_cmp_lg_u32 %[need], 4\n\ts_cbranch_scc1 Lz%=\n\ts_mov_b32 s44, 0x40000\n\tv_lshlrev_b32 v54, 3, %[vo]\n\t"          \
  "Ly%=:\n\tglobal_load_dwordx2 v[52:53], v54, %[pf] sc1\n\ts_waitcnt vmcnt(0)\n\t"                                      \
  "v_cmp_ne_u32 vcc, %[tl], v52\n\tv_cmp_ne_u32 s[42:43], %[th], v53\n\ts_nop 3\n\ts_or_b64 s[42:43], s[42:43], vcc\n\t"  \
  "s_cbranch_scc0 Lz%=\n\ts_sleep 4\n\ts_sub_u32 s44, s44, 1\n\ts_cbranch_scc0 Ly%=\n\tLz%=:\n\t" ATT_POST(8)
#define ATT_ASM_E \
  PRIO(2) \
  ATT_TILE(0, "", "") \
  ATT_TILE(1, "", "") \
  ATT_TILE(2, "", "") \
  ATT_TILE(3, "", "") \
  ATT_TILE(4, "", "") \
  ATT_TILE(5, "", "") \
  ATT_TILE(6, "", "") \
  ATT_WAIT(1, 0, 0) \
  ATT_TILE(7, "", "") \
  ATT_TILE(8, "", "") \
  ATT_TILE(9, "", "") \
  ATT_TILE(10, "", "") \
  ATT_TILE(11, "", "") \
  PRIO(1) \
  ATT_TILE(12, "", "") \
  ATT_TILE(13, "", "") \
  ATT_TILE(14, "", "") \
  ATT_WAIT(2, 4, 0) \
  ATT_TILE(15, "", "") \
  ATT_POST(20) \
  ATT_NEXT \
  ATT_TILE(0, "", "") \
  ATT_WAIT(4, 8, 0) \
  ATT_TILE(1, "s_mov_b64 s[42:43], %[pa]\n\t" WLD(84, -4096) WLD(88, -3072), WLD(92, -2048)) \
  ATT_TILE(2, WLD(96, -1024) WLD(100, 0), WLD(104, 1024)) \
  ATT_TILE(3, WLD(108, 2048) WLD(112, 3072), BUMP(0x2000) WLD(116, -4096)) \
  ATT_TILE(4, WLD(120, -3072) WLD(124, -2048), WLD(128, -1024)) \
  ATT_TILE(5, WLD(132, 0) WLD(136, 1024), WLD(140, 2048)) \
  ATT_TILE(6, WLD(144, 3072), BUMP(0x2000) WLD(148, -4096) WLD(152, -3072)) \
  ATT_TILE(7, WLD(156, -2048), WLD(160, -1024) WLD(164, 0)) \
  ATT_TILE(8, WLD(168, 1024), WLD(172, 2048) WLD(176, 3072)) \
  ATT_TILE(9, BUMP(0x2000) WLD(180, -4096), WLD(184, -3072) WLD(188, -2048)) \
  ATT_TILE(10, WLD(192, -1024), WLD(196, 0) WLD(200, 1024)) \
  ATT_TILE(11, WLD(204, 2048), WLD(208, 3072)) \
  ATT_TILE(12, "s_mov_b64 s[42:43], %[pb]\n\t" WLD(68, -4096) WLD(72, -3072), WLD(76, -2048)) \
  ATT_TILE(13, WLD(80, -1024), WLD(52, 0) WLD(56, 1024)) \
  PRIO(0) \
  ATT_TILE(14, WLD(60, 2048), WLD(64, 3072)) \
  ATT_TILE(15, "", "")
#define ATT_ASM_L \
  PRIO(2) \
  ATT_TILE(0, "", "") \
  ATT_TILE(1, "", "") \
  ATT_TILED \
  ATT_TILE(2, "", "") \
  ATT_WAIT(4, 8, 0) \
  ATT_TILE(3, "s_mov_b64 s[42:43], %[pa]\n\t" WLD(84, -4096) WLD(88, -3072), WLD(92, -2048)) \
  ATT_TILE(4, WLD(96, -1024), WLD(100, 0)) \
  ATT_TILE(5, WLD(104, 1024), WLD(108, 2048)) \
  ATT_TILE(6, WLD(112, 3072), BUMP(0x2000) WLD(116, -4096) WLD(120, -3072)) \
  ATT_TILE(7, WLD(124, -2048), WLD(128, -1024)) \
  ATT_TILE(8, WLD(132, 0), WLD(136, 1024)) \
  ATT_TILE(9, WLD(140, 2048), WLD(144, 3072)) \
  ATT_TILE(10, BUMP(0x2000) WLD(148, -4096) WLD(152, -3072), WLD(156, -2048)) \
  ATT_TILE(11, WLD(160, -1024), WLD(164, 0)) \
  ATT_TILE(12, WLD(168, 1024), WLD(172, 2048)) \
  ATT_TILE(13, WLD(176, 3072), BUMP(0x2000) WLD(180, -4096) WLD(184, -3072)) \
  PRIO(1) \
  ATT_TILE(14, WLD(188, -2048), WLD(192, -1024)) \
  ATT_TILE(15, WLD(196, 0), WLD(200, 1024)) \
  ATT_POST(20) \
  ATT_NEXT \
  ATT_TILE(0, WLD(204, 2048), WLD(208, 3072)) \
  ATT_TILE(1, "", "") \
  ATT_TILE(2, "", "") \
  ATT_TILE(3, "", "") \
  ATT_TILE(4, "", "") \
  ATT_TILE(5, "", "") \
  ATT_WAIT(3, 20, 1) \
  ATT_TILE(6, "s_mov_b64 s[42:43], %[pw]\n\ts_mov_b32 m0, %[lw]\n\ts_nop 0\n\t" WDMA(0) WDMA(1024), WDMA(2048) WDMA(3072)) \
  ATT_TILE(7, BUMP(4096) "s_add_u32 m0, m0, 0x1000\n\ts_nop 0\n\t" WDMA(0), WDMA(1024) WDMA(2048)) \
  ATT_TILE(8, WDMA(3072), BUMP(4096) "s_add_u32 m0, m0, 0x1000\n\ts_nop 0\n\t" WDMA(0) WDMA(1024)) \
  ATT_TILE(9, WDMA(2048) WDMA(3072), BUMP(4096) "s_add_u32 m0, m0, 0x1000\n\ts_nop 0\n\t" WDMA(0)) \
  ATT_TILE(10, WDMA(1024) WDMA(2048), WDMA(3072)) \
  ATT_TILE(11, "s_mov_b64 s[42:43], %[pb]\n\t" WLD(68, -4096) WLD(72, -3072), WLD(76, -2048)) \
  ATT_TILE(12, WLD(80, -1024), WLD(52, 0) WLD(56, 1024)) \
  ATT_TILE(13, WLD(60, 2048), WLD(64, 3072)) \
  PRIO(0) \
  ATT_TILE(14, "", "") \
  ATT_TILE(15, "", "")
struct DnRegs { u32x32 wp[4]; u32x16 d0, d1; u32x8 d2; u32x4 dx; };
#define ATT_OUT [Oa] "=&v"(Oa), [Ob] "=&v"(Ob), [ka] "+v"(kaddr), [va] "+v"(vaddr), "=&{v[68:83]}"(R.d0), "=&{v[52:67]}"(R.d1),          \
    "=&{v[84:115]}"(R.wp[0]), "=&{v[116:147]}"(R.wp[1]), "=&{v[148:179]}"(R.wp[2]), "=&{v[180:211]}"(R.wp[3])
#define ATT_IN [qa] "v"(qa), [qb] "v"(qb), [idx] "v"(idx), [vo] "v"(l16), [pa] "s"(pa + 4096), [pb] "s"(pb + 4096), [sy] "v"(sy), [need] "s"(need)
template <bool EARLY>
DEVI void att_fast(unsigned& kaddr, unsigned& vaddr, u32x4 qa, u32x4 qb, f32x16& Oa, f32x16& Ob, int idx, unsigned sy, int need,
                   const char* pa, const char* pb, const char* pw, unsigned lw, unsigned l16,
                   const char* pf, u64 tag, DnRegs& R) {
  if constexpr (EARLY)
    asm volatile(ATT_HEAD ATT_ASM_E ATT_TAIL : ATT_OUT : ATT_IN : "s42", "s43", "s44", "s45", ATT_CLOB);
  else
    asm volatile(ATT_HEAD ATT_ASM_L ATT_TAIL : ATT_OUT : ATT_IN, [pw] "s"(pw), [lw] "s"(lw),
                   [pf] "s"(pf), [tl] "s"((unsigned)tag), [th] "s"((unsigned)(tag >> 32))
                 : "vcc", "s42", "s43", "s44", "s45", ATT_CLOB);
}
constexpr int ATT_X = 139264;
constexpr int QKV_X = 17408, QKV_W = QKV_X + 65536, QKV_Q = QKV_W + 65536;
constexpr int QKV_ST = QKV_Q + 8192;
constexpr int GU_MF = 159744;
constexpr int GU_R2 = GU_MF + 2048;
constexpr int WARM_PAD = 162304;
constexpr int SY = 163328;
constexpr int S_KV = SY  , S_KP = SY + 12, S_K0 = SY + 16, S_LP = SY + 20, S_H = SY + 88  , S_V = SY + 32  ,
              S_X = SY + 40  , S_OK = SY + 56  , S_WQ = SY + 72, S_ST = SY + 76, S_P = SY + 80, S_PR = SY + 96  ;
static_assert(S_H - (S_X + 4) == 44 && S_H + 4 - S_V == 60, "offsets used in the GU asm");
#define PIN(x) asm volatile("" : "+v"(x))

template <bool B> struct BoolC { static constexpr bool value = B; };
DEVI const char* karg(int i) {
  typedef const char* const __attribute__((address_space(4))) * KP;
  KP p = (KP)__builtin_amdgcn_kernarg_segment_ptr();
  asm volatile("" : "+s"(p));
  return p[i];
}
enum { A_X, A_WQ, A_WK, A_WV, A_WO, A_WG, A_WU, A_WD, A_WS, A_OUT };
struct Ctx { char* ws; LAS char* sm; rsrc_t rs; u64 tag; int w_, g_, r_; };
#define CTX_IDS() LAS char* sm = c.sm; char* ws = c.ws; const rsrc_t rs = c.rs; const int w_ = c.w_, g_ = c.g_, r_ = c.r_; \
  (void)ws; (void)rs; PHASE_IDS()

DEVI bool att_bound_ok(u32x4 qa, u32x4 qb, float km) {
  float qna = 0.f, qnb = 0.f;
#pragma unroll
  for (int j = 0; j < 4; j++) { qna = dot2(qa[j], qa[j], qna); qnb = dot2(qb[j], qb[j], qnb); }
  const float kmax = red_wave<OpMax>(km);
  return __all(fmaxf(red32<OpAdd>(qna), red32<OpAdd>(qnb)) * kmax <= FAST_BOUND * FAST_BOUND);
}

template <bool EARLY>
DEVI void att_finish(const Ctx& c, int layer, const f32x16& Oa, const f32x16& Ob, float ma, float mb) {
  CTX_IDS();
  LAS char* xb = sm + ATT_X + (w & 3) * 5120 + l16;
  if constexpr (EARLY) {
    *(LAS f32x4*)(xb) = f32x4{Oa[0], Oa[1], Oa[2], Oa[3]}; *(LAS f32x4*)(xb + 1024) = f32x4{Oa[4], Oa[5], Oa[6], Oa[7]};
    *(LAS f32x4*)(xb + 2048) = f32x4{Ob[0], Ob[1], Ob[2], Ob[3]}; *(LAS f32x4*)(xb + 3072) = f32x4{Ob[4], Ob[5], Ob[6], Ob[7]};
    *(LAS f32x4*)(xb + 4096) = f32x4{Oa[8], ma, Ob[8], mb};
    lds_bar();
  } else {
    bar();
    const f32x4 pl = lds_rdf(xb, 4096), pa0 = lds_rdf(xb, 0), pa1 = lds_rdf(xb, 1024), pb0 = lds_rdf(xb, 2048), pb1 = lds_rdf(xb, 3072);
    const unsigned oo = l16 + gb + G_O + ((m4 * 2 + (w & 1)) * 32 + nt8 * 4 + (w & 2)) * 1024;
    auto fin = [&](const f32x16& O, float m, f32x4 p0, f32x4 p1, float pl_, float pm) {
      const float mx = fmaxf(m, pm);
      float fa = __builtin_amdgcn_exp2f(m - mx), fb = __builtin_amdgcn_exp2f(pm - mx);
      const float inv = __builtin_amdgcn_rcpf(O[8] * fa + pl_ * fb);
      fa *= inv; fb *= inv;
      const f32x4 r0 = f32x4{O[0], O[1], O[2], O[3]} * fa + p0 * fb, r1 = f32x4{O[4], O[5], O[6], O[7]} * fa + p1 * fb;
      return u32x4{pack2(r0[0], r0[1]), pack2(r0[2], r0[3]), pack2(r1[0], r1[1]), pack2(r1[2], r1[3])};
    };
    st16(rs, oo, 0, fin(Oa, ma, pa0, pa1, pl[0], pl[1]));
    st16(rs, oo, 1024, fin(Ob, mb, pb0, pb1, pl[2], pl[3]));
  }
}

template <class T> DEVI u32x4 frag(const T& t, int i) { return u32x4{t[4 * i], t[4 * i + 1], t[4 * i + 2], t[4 * i + 3]}; }
DEVI void gu_colsum(const u32x32 (&wp)[4], u32x4& cg, u32x4& cu) {
  const u32x4 ones = {0x3f803f80u, 0x3f803f80u, 0x3f803f80u, 0x3f803f80u};
  f32x4 sg = {}, su = {};
#pragma unroll
  for (int ks = 0; ks < 16; ks++) { sg = mfma16(ones, frag(wp[ks >> 2], (2 * ks) & 7), sg); su = mfma16(ones, frag(wp[ks >> 2], (2 * ks + 1) & 7), su); }
  const bool on = lane_id() < 16;
  const float g0 = on ? sg[0] : 0.f, u0 = on ? su[0] : 0.f;
  cg[1] = pack2(g0, 0.f); cg[0] = pack2(g0, g0 - bf_lo(cg[1]));
  cu[1] = pack2(u0, 0.f); cu[0] = pack2(u0, u0 - bf_lo(cu[1]));
  cg[2] = cg[3] = cu[2] = cu[3] = cg[1];
  PIN(cg); PIN(cu);
  SCHED();
}
DEVI u32x4 phase_wo(const Ctx& c, int layer, const DnRegs& R, u32x4& cg, u32x4& cu) {
  CTX_IDS();
  const int tt = w >> 2, cw = w & 3;
  int oa = (2 + 4 * tt) * KVG + (lane >> 5) * 1024 + ((lane >> 4) & 1) * 512 + (lane & 15) * 16, wa = (cw >> 1) * 4 * KVG + (cw & 1) * 16384 + l16;
  unsigned xo = l16 + gb + ((m4 * 2 + tt) * 32 + nt8 * 4 + cw) * 1024;
  unsigned so = ((lane & 31) * 2 + (lane >> 5)) * 4 + gb + G_ST + (r * 8 + w) * 256;
  PIN(oa); PIN(wa); PIN(xo); PIN(so);
  u32x4 xr;
  if (layer) xr = ld16(rs, xo, G_X2);
  else xr = __builtin_amdgcn_raw_buffer_load_b128(__builtin_amdgcn_make_buffer_rsrc((void*)karg(A_X), 0, 2048 * 1024, 0x00020000),
                                                  (lane & 31) * 1024 + (lane >> 5) * 16, (m64 * 64 + tt * 32) * 1024 + nt8 * 128 + cw * 32, 0);
  if (tt) {
    WAIT_VM(1);
    set_flag1(ws + (gb + G_FO + (r * 4 + cw) * FL), opaque_t(c.tag + layer));
    if (BACK & 1) {
      const char* ob = ws + gb + G_O + ((m4 * 2 + (w & 1)) * 32 + nt8 * 4 + (w & 2)) * 1024;
      glds(l16, ob, smb + WARM_PAD); glds(l16, ob + 1024, smb + WARM_PAD);
    }
    STAMP(10);
  } else __builtin_amdgcn_s_sleep(O_NAP);
  gu_colsum(R.wp, cg, cu);
  poll(ws + gb + G_FO + ((r & ~7) + w) * 4 * FL, (lane & 3) * FL, c.tag + layer);
  STAMP(11);
  glds4(l16, ws + gb + G_O + (m4 * 64 + 4 * w) * 1024, smb + 2 * KVG + 4 * w * 1024);
  glds4(l16, ws + gb + G_O + (m4 * 64 + 32 + 4 * w) * 1024, smb + 6 * KVG + 4 * w * 1024);
  u32x4 wf[16];
#pragma unroll
  for (int ks = 0; ks < WO_PRE; ks++) wf[ks] = lds_rd(sm, wa + ks * 1024);
  asm volatile("s_waitcnt vmcnt(0)" : "+v"(xr) :: "memory");
  bar();
  STAMP(12);
  f32x4 y0 = {}, y1 = {};
  u32x4 of[16][2];
#pragma unroll
  for (int ks = 0; ks < 3; ks++) { of[ks][0] = lds_rd(sm, oa + ks * 2048); of[ks][1] = lds_rd(sm, oa + ks * 2048 + 256); }
#pragma unroll
  for (int ks = 0; ks < 16; ks++) {
    if (ks + 3 < 16) { of[ks + 3][0] = lds_rd(sm, oa + (ks + 3) * 2048); of[ks + 3][1] = lds_rd(sm, oa + (ks + 3) * 2048 + 256); }
    if (ks >= 2 && ks + WO_PRE - 2 < 16) wf[ks + WO_PRE - 2] = lds_rd(sm, wa + (ks + WO_PRE - 2) * 1024);
    y0 = mfma16(wf[ks], of[ks][0], y0); y1 = mfma16(wf[ks], of[ks][1], y1);
    SCHED();
  }
  float v[8];
#pragma unroll
  for (int j = 0; j < 4; j++) {
    const float ya = y0[j], yb = y1[j];
    const auto e = __builtin_amdgcn_permlane16_swap(__builtin_bit_cast(unsigned, ya), __builtin_bit_cast(unsigned, yb), false, false);
    v[j] = add1(__builtin_bit_cast(float, (unsigned)e[0]), (j & 1) ? bf_hi(xr[j >> 1]) : bf_lo(xr[j >> 1]));
    v[4 + j] = add1(__builtin_bit_cast(float, (unsigned)e[1]), (j & 1) ? bf_hi(xr[2 + (j >> 1)]) : bf_lo(xr[2 + (j >> 1)]));
  }
  const u32x4 x1 = {pack2(v[0], v[1]), pack2(v[2], v[3]), pack2(v[4], v[5]), pack2(v[6], v[7])};
  st16(rs, xo, G_X1, x1);
  float s = dot2(x1[0], 0x3f803f80u, dot2(x1[1], 0x3f803f80u, dot2(x1[2], 0x3f803f80u, dot2(x1[3], 0x3f803f80u, 0.f))));
  float ss = dot2(x1[0], x1[0], dot2(x1[1], x1[1], dot2(x1[2], x1[2], dot2(x1[3], x1[3], 0.f))));
  const auto e = __builtin_amdgcn_permlane32_swap(__builtin_bit_cast(unsigned, s), __builtin_bit_cast(unsigned, ss), false, false);
  const float t = add1(__builtin_bit_cast(float, (unsigned)e[0]), __builtin_bit_cast(float, (unsigned)e[1]));
  __builtin_amdgcn_raw_buffer_store_b32(__builtin_bit_cast(unsigned, t), rs, so, 0, 16);
  WAIT_VM(0);
  if (BACK & 2) glds(xo, ws + G_X1, smb + WARM_PAD);
  bar();
  if (w == 0) set_flag1(ws + (gb + G_FX + r * FL), opaque_t(c.tag + layer));
  STAMP(13);
  return x1;
}

DEVI void gu_fetch(const Ctx& c, int layer) {
  CTX_IDS();
  const int e = w & 3, b0 = (r & ~15) + (e >> 1) * 8;
  if (w >= 4) {
    const char* xg = ws + gb + G_X1 + (((r >> 4) * 4 + (e >> 1) * 2) * 32 + (e & 1) * 8) * 1024;
    const unsigned xl = smb + (e >> 1) * 32768 + (e & 1) * 8192;
    unsigned sx = smb + S_X;
    PIN(sx);
    __builtin_amdgcn_s_sleep(X_NAP);
    poll(ws + gb + G_FX + b0 * FL, (lane & 7) * FL, c.tag + layer);
    STAMP(17);
#pragma unroll
    for (int i = 0; i < 8; i++) {
      glds4(l16, xg + (i >> 1) * 16384 + (i & 1) * 4096, xl + (i >> 2) * 65536 + ((i >> 1) & 1) * 16384 + (i & 1) * 4096);
      if (i == 2) asm volatile("s_waitcnt vmcnt(4)\n\ts_mov_b64 exec, 1\n\tds_inc_u32 %0, %0\n\ts_mov_b64 exec, -1" :: "v"(sx) : "memory");
    }
    asm volatile("s_waitcnt vmcnt(16)\n\ts_mov_b64 exec, 1\n\tds_inc_u32 %0, %0 offset:4\n\ts_mov_b64 exec, -1" :: "v"(sx) : "memory");
  }
  if (e == 3) {
    const int tt = w >> 2;
    unsigned so = (lane >> 5) * 16384 + (lane & 31) * 8;
    PIN(so);
    lds_wait<1>(sm + S_X, 4 * layer + 4);
    STAMP(18);
    f32x2 t[2][16];
#pragma unroll
    for (int i = 0; i < 8; i++)
      asm volatile("global_load_dwordx2 %0, %4, %5 sc1\n\tglobal_load_dwordx2 %1, %4, %5 offset:256 sc1\n\t"
                   "global_load_dwordx2 %2, %4, %5 offset:512 sc1\n\tglobal_load_dwordx2 %3, %4, %5 offset:768 sc1"
                   : "=&v"(t[i >> 2][(i & 3) * 4]), "=&v"(t[i >> 2][(i & 3) * 4 + 1]), "=&v"(t[i >> 2][(i & 3) * 4 + 2]), "=&v"(t[i >> 2][(i & 3) * 4 + 3])
                   : "v"(so), "s"(ws + gb + G_ST + (((r & ~15) + i) * 2 + tt) * 1024) : "memory");
    float s = 0.f, ss = 0.f;
#pragma unroll
    for (int k = 0; k < 2; k++) {
      asm volatile("s_waitcnt vmcnt(0)" : "+v"(t[k][0]), "+v"(t[k][1]), "+v"(t[k][2]), "+v"(t[k][3]), "+v"(t[k][4]), "+v"(t[k][5]), "+v"(t[k][6]),
                   "+v"(t[k][7]), "+v"(t[k][8]), "+v"(t[k][9]), "+v"(t[k][10]), "+v"(t[k][11]), "+v"(t[k][12]), "+v"(t[k][13]), "+v"(t[k][14]),
                   "+v"(t[k][15]) :: "memory");
#pragma unroll
      for (int i = 0; i < 16; i += 2) { s = add1(s, add1(t[k][i][0], t[k][i + 1][0])); ss = add1(ss, add1(t[k][i][1], t[k][i + 1][1])); }
    }
    float invd = 1.f / D_;
    asm volatile("" : "+s"(invd));
    const float nm = -s * invd, var = mul1(ss, invd) - nm * nm;
    const unsigned hi = pack2(nm, nm);
    *(LAS u32x4*)(sm + GU_MF + (tt * 64 + lane) * 16) = u32x4{hi, pack2(nm - bf_lo(hi), 0.f), fresh(0), fresh(0)};
    *(LAS float*)(sm + GU_R2 + (tt * 64 + lane) * 4) = __builtin_amdgcn_rcpf(fmaxf(var, 0.f) + LN_EPS);
    if (tt == 0) lds_inc(sm + S_X + 8);
  }
}

DEVI void h_flag(const Ctx& c, int layer, int tt) {
  CTX_IDS();
  lds_wait<1>(sm + S_H + tt * 4, 7 * layer + 7);
  set_flag1(ws + gb + G_FH + (tt * 32 + r) * FL, opaque_t(c.tag + layer));
}
DEVI void h_poll(const Ctx& c, int layer, int tt) {
  CTX_IDS();
  poll(ws + gb + G_FH + (tt * 32 + (r & ~15)) * FL, (lane & 15) * FL, c.tag + layer);
  lds_post(sm + S_V + tt * 4, layer + 1);
}

#define GU_ASM_A \
  "s_waitcnt lgkmcnt(0)\n\t" "s_mov_b64 s[42:43], %[pd]\n\t" "s_cmp_eq_u32 %[wv], 7\n\t" "s_cbranch_scc1 Lseven%=\n\t" "s_cmp_ge_u32 %[wv], 4\n\t" \
  "s_cselect_b64 vcc, -1, 0\n\t" "ds_read_b128 v[244:247], %[lr] offset:0\n\t" "ds_read_b128 v[248:251], %[lr] offset:256\n\t" \
  "ds_read_b128 v[252:255], %[lr] offset:32768\n\t" "ds_read_b128 v[32:35], %[lr] offset:33024\n\t" "s_waitcnt lgkmcnt(3)\n\t" \
  "v_mfma_f32_16x16x32_bf16 v[212:215], v[84:87], v[244:247], 0\n\t" "ds_read_b128 v[36:39], %[lr] offset:2048\n\t" \
  "v_mfma_f32_16x16x32_bf16 v[216:219], v[88:91], v[244:247], 0\n\t" "s_waitcnt lgkmcnt(3)\n\t" \
  "v_mfma_f32_16x16x32_bf16 v[220:223], v[84:87], v[248:251], 0\n\t" "ds_read_b128 v[40:43], %[lr] offset:2304\n\t" \
  "v_mfma_f32_16x16x32_bf16 v[224:227], v[88:91], v[248:251], 0\n\t" "s_waitcnt lgkmcnt(3)\n\t" \
  "v_mfma_f32_16x16x32_bf16 v[228:231], v[84:87], v[252:255], 0\n\t" "ds_read_b128 v[244:247], %[lr] offset:34816\n\t" \
  "v_mfma_f32_16x16x32_bf16 v[232:235], v[88:91], v[252:255], 0\n\t" "s_waitcnt lgkmcnt(3)\n\t" \
  "v_mfma_f32_16x16x32_bf16 v[236:239], v[84:87], v[32:35], 0\n\t" "ds_read_b128 v[248:251], %[lr] offset:35072\n\t" \
  "v_mfma_f32_16x16x32_bf16 v[240:243], v[88:91], v[32:35], 0\n\t" "s_waitcnt lgkmcnt(3)\n\t" \
  "v_mfma_f32_16x16x32_bf16 v[212:215], v[92:95], v[36:39], v[212:215]\n\t" "ds_read_b128 v[252:255], %[lr] offset:4096\n\t" \
  "v_mfma_f32_16x16x32_bf16 v[216:219], v[96:99], v[36:39], v[216:219]\n\t" "s_waitcnt lgkmcnt(3)\n\t" \
  "v_mfma_f32_16x16x32_bf16 v[220:223], v[92:95], v[40:43], v[220:223]\n\t" "ds_read_b128 v[32:35], %[lr] offset:4352\n\t" \
  "v_mfma_f32_16x16x32_bf16 v[224:227], v[96:99], v[40:43], v[224:227]\n\t" "s_waitcnt lgkmcnt(3)\n\t" \
  "v_mfma_f32_16x16x32_bf16 v[228:231], v[92:95], v[244:247], v[228:231]\n\t" "ds_read_b128 v[36:39], %[lr] offset:36864\n\t" \
  "v_mfma_f32_16x16x32_bf16 v[232:235], v[96:99], v[244:247], v[232:235]\n\t" "s_waitcnt lgkmcnt(3)\n\t" \
  "v_mfma_f32_16x16x32_bf16 v[236:239], v[92:95], v[248:251], v[236:239]\n\t" "ds_read_b128 v[40:43], %[lr] offset:37120\n\t" \
  "v_mfma_f32_16x16x32_bf16 v[240:243], v[96:99], v[248:251], v[240:243]\n\t" "s_waitcnt lgkmcnt(3)\n\t" \
  "v_mfma_f32_16x16x32_bf16 v[212:215], v[100:103], v[252:255], v[212:215]\n\t" "ds_read_b128 v[244:247], %[lr] offset:6144\n\t" \
  "v_mfma_f32_16x16x32_bf16 v[216:219], v[104:107], v[252:255], v[216:219]\n\t" "s_waitcnt lgkmcnt(3)\n\t" \
  "v_mfma_f32_16x16x32_bf16 v[220:223], v[100:103], v[32:35], v[220:223]\n\t" "ds_read_b128 v[248:251], %[lr] offset:6400\n\t" \
  "v_mfma_f32_16x16x32_bf16 v[224:227], v[104:107], v[32:35], v[224:227]\n\t" "s_waitcnt lgkmcnt(3)\n\t" \
  "v_mfma_f32_16x16x32_bf16 v[228:231], v[100:103], v[36:39], v[228:231]\n\t" "ds_read_b128 v[252:255], %[lr] offset:38912\n\t" \
  "v_mfma_f32_16x16x32_bf16 v[232:235], v[104:107], v[36:39], v[232:235]\n\t" "s_waitcnt lgkmcnt(3)\n\t" \
  "v_mfma_f32_16x16x32_bf16 v[236:239], v[100:103], v[40:43], v[236:239]\n\t" "ds_read_b128 v[32:35], %[lr] offset:39168\n\t" \
  "v_mfma_f32_16x16x32_bf16 v[240:243], v[104:107], v[40:43], v[240:243]\n\t" "s_waitcnt lgkmcnt(3)\n\t" \
  "v_mfma_f32_16x16x32_bf16 v[212:215], v[108:111], v[244:247], v[212:215]\n\t" "ds_read_b128 v[36:39], %[lr] offset:8192\n\t" \
  "v_mfma_f32_16x16x32_bf16 v[216:219], v[112:115], v[244:247], v[216:219]\n\t" "s_waitcnt lgkmcnt(3)\n\t" \
  "v_mfma_f32_16x16x32_bf16 v[220:223], v[108:111], v[248:251], v[220:223]\n\t" "ds_read_b128 v[40:43], %[lr] offset:8448\n\t" \
  "v_mfma_f32_16x16x32_bf16 v[224:227], v[112:115], v[248:251], v[224:227]\n\t" "s_waitcnt lgkmcnt(3)\n\t" \
  "v_mfma_f32_16x16x32_bf16 v[228:231], v[108:111], v[252:255], v[228:231]\n\t" "ds_read_b128 v[244:247], %[lr] offset:40960\n\t" \
  "v_mfma_f32_16x16x32_bf16 v[232:235], v[112:115], v[252:255], v[232:235]\n\t" "s_waitcnt lgkmcnt(3)\n\t" \
  "v_mfma_f32_16x16x32_bf16 v[236:239], v[108:111], v[32:35], v[236:239]\n\t" "ds_read_b128 v[248:251], %[lr] offset:41216\n\t" \
  "v_mfma_f32_16x16x32_bf16 v[240:243], v[112:115], v[32:35], v[240:243]\n\t" "s_waitcnt lgkmcnt(3)\n\t" \
  "v_mfma_f32_16x16x32_bf16 v[212:215], v[116:119], v[36:39], v[212:215]\n\t" "ds_read_b128 v[252:255], %[lr] offset:10240\n\t" \
  "v_mfma_f32_16x16x32_bf16 v[216:219], v[120:123], v[36:39], v[216:219]\n\t" "s_waitcnt lgkmcnt(3)\n\t" \
  "v_mfma_f32_16x16x32_bf16 v[220:223], v[116:119], v[40:43], v[220:223]\n\t" "ds_read_b128 v[32:35], %[lr] offset:10496\n\t" \
  "v_mfma_f32_16x16x32_bf16 v[224:227], v[120:123], v[40:43], v[224:227]\n\t" "s_waitcnt lgkmcnt(3)\n\t" \
  "v_mfma_f32_16x16x32_bf16 v[228:231], v[116:119], v[244:247], v[228:231]\n\t" "ds_read_b128 v[36:39], %[lr] offset:43008\n\t" \
  "v_mfma_f32_16x16x32_bf16 v[232:235], v[120:123], v[244:247], v[232:235]\n\t" "s_waitcnt lgkmcnt(3)\n\t" \
  "v_mfma_f32_16x16x32_bf16 v[236:239], v[116:119], v[248:251], v[236:239]\n\t" "ds_read_b128 v[40:43], %[lr] offset:43264\n\t" \
  "v_mfma_f32_16x16x32_bf16 v[240:243], v[120:123], v[248:251], v[240:243]\n\t" "s_waitcnt lgkmcnt(3)\n\t" \
  "v_mfma_f32_16x16x32_bf16 v[212:215], v[124:127], v[252:255], v[212:215]\n\t" "ds_read_b128 v[244:247], %[lr] offset:12288\n\t" \
  "v_mfma_f32_16x16x32_bf16 v[216:219], v[128:131], v[252:255], v[216:219]\n\t" "s_waitcnt lgkmcnt(3)\n\t" \
  "v_mfma_f32_16x16x32_bf16 v[220:223], v[124:127], v[32:35], v[220:223]\n\t" "ds_read_b128 v[248:251], %[lr] offset:12544\n\t" \
  "v_mfma_f32_16x16x32_bf16 v[224:227], v[128:131], v[32:35], v[224:227]\n\t" "s_waitcnt lgkmcnt(3)\n\t" \
  "v_mfma_f32_16x16x32_bf16 v[228:231], v[124:127], v[36:39], v[228:231]\n\t" "ds_read_b128 v[252:255], %[lr] offset:45056\n\t" \
  "v_mfma_f32_16x16x32_bf16 v[232:235], v[128:131], v[36:39], v[232:235]\n\t" "s_waitcnt lgkmcnt(3)\n\t" \
  "v_mfma_f32_16x16x32_bf16 v[236:239], v[124:127], v[40:43], v[236:239]\n\t" "ds_read_b128 v[32:35], %[lr] offset:45312\n\t" \
  "v_mfma_f32_16x16x32_bf16 v[240:243], v[128:131], v[40:43], v[240:243]\n\t" "s_waitcnt lgkmcnt(3)\n\t" \
  "v_mfma_f32_16x16x32_bf16 v[212:215], v[132:135], v[244:247], v[212:215]\n\t" "ds_read_b128 v[36:39], %[lr] offset:14336\n\t" \
  "v_mfma_f32_16x16x32_bf16 v[216:219], v[136:139], v[244:247], v[216:219]\n\t" "s_waitcnt lgkmcnt(3)\n\t" \
  "v_mfma_f32_16x16x32_bf16 v[220:223], v[132:135], v[248:251], v[220:223]\n\t" "ds_read_b128 v[40:43], %[lr] offset:14592\n\t" \
  "v_mfma_f32_16x16x32_bf16 v[224:227], v[136:139], v[248:251], v[224:227]\n\t" "s_waitcnt lgkmcnt(3)\n\t" \
  "v_mfma_f32_16x16x32_bf16 v[228:231], v[132:135], v[252:255], v[228:231]\n\t" "ds_read_b128 v[244:247], %[lr] offset:47104\n\t" \
  "v_mfma_f32_16x16x32_bf16 v[232:235], v[136:139], v[252:255], v[232:235]\n\t" "s_waitcnt lgkmcnt(3)\n\t" \
  "v_mfma_f32_16x16x32_bf16 v[236:239], v[132:135], v[32:35], v[236:239]\n\t" "ds_read_b128 v[248:251], %[lr] offset:47360\n\t" \
  "v_mfma_f32_16x16x32_bf16 v[240:243], v[136:139], v[32:35], v[240:243]\n\t" "Lxa%=:\n\t" "ds_read_b32 v252, %[sv] offset:0\n\t" \
  "s_waitcnt lgkmcnt(0)\n\t" "v_readfirstlane_b32 s40, v252\n\t" "s_nop 3\n\t" "s_cmp_ge_i32 s40, %[nx]\n\t" "s_cbranch_scc1 Lxb%=\n\t" \
  "s_sleep 1\n\t" "s_branch Lxa%=\n\t" "Lxb%=:\n\t" "s_waitcnt lgkmcnt(3)\n\t" \
  "v_mfma_f32_16x16x32_bf16 v[212:215], v[140:143], v[36:39], v[212:215]\n\t" "ds_read_b128 v[252:255], %[lr] offset:16384\n\t" \
  "v_mfma_f32_16x16x32_bf16 v[216:219], v[144:147], v[36:39], v[216:219]\n\t" "s_waitcnt lgkmcnt(3)\n\t" \
  "v_mfma_f32_16x16x32_bf16 v[220:223], v[140:143], v[40:43], v[220:223]\n\t" "ds_read_b128 v[32:35], %[lr] offset:16640\n\t" \
  "v_mfma_f32_16x16x32_bf16 v[224:227], v[144:147], v[40:43], v[224:227]\n\t" "s_waitcnt lgkmcnt(3)\n\t" \
  "v_mfma_f32_16x16x32_bf16 v[228:231], v[140:143], v[244:247], v[228:231]\n\t" "ds_read_b128 v[36:39], %[lr] offset:49152\n\t" \
  "v_mfma_f32_16x16x32_bf16 v[232:235], v[144:147], v[244:247], v[232:235]\n\t" "s_waitcnt lgkmcnt(3)\n\t" \
  "v_mfma_f32_16x16x32_bf16 v[236:239], v[140:143], v[248:251], v[236:239]\n\t" "ds_read_b128 v[40:43], %[lr] offset:49408\n\t" \
  "v_mfma_f32_16x16x32_bf16 v[240:243], v[144:147], v[248:251], v[240:243]\n\t" "s_waitcnt lgkmcnt(3)\n\t" \
  "v_mfma_f32_16x16x32_bf16 v[212:215], v[148:151], v[252:255], v[212:215]\n\t" "ds_read_b128 v[244:247], %[lr] offset:18432\n\t" \
  "v_mfma_f32_16x16x32_bf16 v[216:219], v[152:155], v[252:255], v[216:219]\n\t" "s_waitcnt lgkmcnt(3)\n\t" \
  "v_mfma_f32_16x16x32_bf16 v[220:223], v[148:151], v[32:35], v[220:223]\n\t" "ds_read_b128 v[248:251], %[lr] offset:18688\n\t" \
  "v_mfma_f32_16x16x32_bf16 v[224:227], v[152:155], v[32:35], v[224:227]\n\t" "s_waitcnt lgkmcnt(3)\n\t" \
  "v_mfma_f32_16x16x32_bf16 v[228:231], v[148:151], v[36:39], v[228:231]\n\t" "ds_read_b128 v[252:255], %[lr] offset:51200\n\t" \
  "v_mfma_f32_16x16x32_bf16 v[232:235], v[152:155], v[36:39], v[232:235]\n\t" "s_waitcnt lgkmcnt(3)\n\t" \
  "v_mfma_f32_16x16x32_bf16 v[236:239], v[148:151], v[40:43], v[236:239]\n\t" "ds_read_b128 v[32:35], %[lr] offset:51456\n\t" \
  "v_mfma_f32_16x16x32_bf16 v[240:243], v[152:155], v[40:43], v[240:243]\n\t" "s_cbranch_vccz Lp%=\n\t" "s_waitcnt vmcnt(0)\n\t" \
  "s_mov_b64 exec, 1\n\t" "ds_inc_u32 %[sv], %[sv] offset:8\n\t" "s_mov_b64 exec, -1\n\t" "s_waitcnt lgkmcnt(0)\n\t" "Lp%=:\n\t" \
  "s_waitcnt lgkmcnt(3)\n\t" "v_mfma_f32_16x16x32_bf16 v[212:215], v[156:159], v[244:247], v[212:215]\n\t" \
  "ds_read_b128 v[36:39], %[lr] offset:20480\n\t" "v_mfma_f32_16x16x32_bf16 v[216:219], v[160:163], v[244:247], v[216:219]\n\t" \
  "s_waitcnt lgkmcnt(3)\n\t" "v_mfma_f32_16x16x32_bf16 v[220:223], v[156:159], v[248:251], v[220:223]\n\t" \
  "ds_read_b128 v[40:43], %[lr] offset:20736\n\t" "v_mfma_f32_16x16x32_bf16 v[224:227], v[160:163], v[248:251], v[224:227]\n\t" \
  "s_waitcnt lgkmcnt(3)\n\t" "v_mfma_f32_16x16x32_bf16 v[228:231], v[156:159], v[252:255], v[228:231]\n\t" \
  "ds_read_b128 v[244:247], %[lr] offset:53248\n\t" "v_mfma_f32_16x16x32_bf16 v[232:235], v[160:163], v[252:255], v[232:235]\n\t" \
  "s_waitcnt lgkmcnt(3)\n\t" "v_mfma_f32_16x16x32_bf16 v[236:239], v[156:159], v[32:35], v[236:239]\n\t" \
  "ds_read_b128 v[248:251], %[lr] offset:53504\n\t" "v_mfma_f32_16x16x32_bf16 v[240:243], v[160:163], v[32:35], v[240:243]\n\t" \
  "s_waitcnt lgkmcnt(3)\n\t" "v_mfma_f32_16x16x32_bf16 v[212:215], v[164:167], v[36:39], v[212:215]\n\t" \
  "ds_read_b128 v[252:255], %[lr] offset:22528\n\t" "v_mfma_f32_16x16x32_bf16 v[216:219], v[168:171], v[36:39], v[216:219]\n\t" \
  "s_waitcnt lgkmcnt(3)\n\t" "v_mfma_f32_16x16x32_bf16 v[220:223], v[164:167], v[40:43], v[220:223]\n\t" \
  "ds_read_b128 v[32:35], %[lr] offset:22784\n\t" "v_mfma_f32_16x16x32_bf16 v[224:227], v[168:171], v[40:43], v[224:227]\n\t" \
  "global_load_dwordx4 v[44:47], %[vo], s[42:43] offset:-4096 sc1\n\t" "s_waitcnt lgkmcnt(3)\n\t" \
  "v_mfma_f32_16x16x32_bf16 v[228:231], v[164:167], v[244:247], v[228:231]\n\t" "ds_read_b128 v[36:39], %[lr] offset:55296\n\t" \
  "v_mfma_f32_16x16x32_bf16 v[232:235], v[168:171], v[244:247], v[232:235]\n\t" "s_waitcnt lgkmcnt(3)\n\t" \
  "v_mfma_f32_16x16x32_bf16 v[236:239], v[164:167], v[248:251], v[236:239]\n\t" "ds_read_b128 v[40:43], %[lr] offset:55552\n\t" \
  "v_mfma_f32_16x16x32_bf16 v[240:243], v[168:171], v[248:251], v[240:243]\n\t" "s_waitcnt lgkmcnt(3)\n\t" \
  "v_mfma_f32_16x16x32_bf16 v[212:215], v[172:175], v[252:255], v[212:215]\n\t" "ds_read_b128 v[244:247], %[lr] offset:24576\n\t" \
  "v_mfma_f32_16x16x32_bf16 v[216:219], v[176:179], v[252:255], v[216:219]\n\t" "s_waitcnt lgkmcnt(3)\n\t" \
  "v_mfma_f32_16x16x32_bf16 v[220:223], v[172:175], v[32:35], v[220:223]\n\t" "ds_read_b128 v[248:251], %[lr] offset:24832\n\t" \
  "v_mfma_f32_16x16x32_bf16 v[224:227], v[176:179], v[32:35], v[224:227]\n\t" "global_load_dwordx4 v[48:51], %[vo], s[42:43] offset:-3072 sc1\n\t" \
  "s_waitcnt lgkmcnt(3)\n\t" "v_mfma_f32_16x16x32_bf16 v[228:231], v[172:175], v[36:39], v[228:231]\n\t" \
  "ds_read_b128 v[252:255], %[lr] offset:57344\n\t" "v_mfma_f32_16x16x32_bf16 v[232:235], v[176:179], v[36:39], v[232:235]\n\t" \
  "s_waitcnt lgkmcnt(3)\n\t" "v_mfma_f32_16x16x32_bf16 v[236:239], v[172:175], v[40:43], v[236:239]\n\t" \
  "ds_read_b128 v[32:35], %[lr] offset:57600\n\t" "v_mfma_f32_16x16x32_bf16 v[240:243], v[176:179], v[40:43], v[240:243]\n\t" \
  "s_waitcnt lgkmcnt(3)\n\t" "v_mfma_f32_16x16x32_bf16 v[212:215], v[180:183], v[244:247], v[212:215]\n\t" \
  "ds_read_b128 v[36:39], %[lr] offset:26624\n\t" "v_mfma_f32_16x16x32_bf16 v[216:219], v[184:187], v[244:247], v[216:219]\n\t" \
  "s_waitcnt lgkmcnt(3)\n\t" "v_mfma_f32_16x16x32_bf16 v[220:223], v[180:183], v[248:251], v[220:223]\n\t" \
  "ds_read_b128 v[40:43], %[lr] offset:26880\n\t" "v_mfma_f32_16x16x32_bf16 v[224:227], v[184:187], v[248:251], v[224:227]\n\t" \
  "s_waitcnt lgkmcnt(3)\n\t" "v_mfma_f32_16x16x32_bf16 v[228:231], v[180:183], v[252:255], v[228:231]\n\t" \
  "ds_read_b128 v[244:247], %[lr] offset:59392\n\t" "v_mfma_f32_16x16x32_bf16 v[232:235], v[184:187], v[252:255], v[232:235]\n\t" \
  "s_waitcnt lgkmcnt(3)\n\t" "v_mfma_f32_16x16x32_bf16 v[236:239], v[180:183], v[32:35], v[236:239]\n\t" \
  "ds_read_b128 v[248:251], %[lr] offset:59648\n\t" "v_mfma_f32_16x16x32_bf16 v[240:243], v[184:187], v[32:35], v[240:243]\n\t" \
  "s_waitcnt lgkmcnt(3)\n\t" "v_mfma_f32_16x16x32_bf16 v[212:215], v[188:191], v[36:39], v[212:215]\n\t" \
  "ds_read_b128 v[252:255], %[lr] offset:28672\n\t" "v_mfma_f32_16x16x32_bf16 v[216:219], v[192:195], v[36:39], v[216:219]\n\t" \
  "s_waitcnt lgkmcnt(3)\n\t" "v_mfma_f32_16x16x32_bf16 v[220:223], v[188:191], v[40:43], v[220:223]\n\t" \
  "ds_read_b128 v[32:35], %[lr] offset:28928\n\t" "v_mfma_f32_16x16x32_bf16 v[224:227], v[192:195], v[40:43], v[224:227]\n\t" \
  "s_waitcnt lgkmcnt(3)\n\t" "v_mfma_f32_16x16x32_bf16 v[228:231], v[188:191], v[244:247], v[228:231]\n\t" \
  "ds_read_b128 v[36:39], %[lr] offset:61440\n\t" "v_mfma_f32_16x16x32_bf16 v[232:235], v[192:195], v[244:247], v[232:235]\n\t" \
  "s_waitcnt lgkmcnt(3)\n\t" "v_mfma_f32_16x16x32_bf16 v[236:239], v[188:191], v[248:251], v[236:239]\n\t" \
  "ds_read_b128 v[40:43], %[lr] offset:61696\n\t" "v_mfma_f32_16x16x32_bf16 v[240:243], v[192:195], v[248:251], v[240:243]\n\t" \
  "s_waitcnt lgkmcnt(3)\n\t" "v_mfma_f32_16x16x32_bf16 v[212:215], v[196:199], v[252:255], v[212:215]\n\t" \
  "ds_read_b128 v[244:247], %[lr] offset:30720\n\t" "v_mfma_f32_16x16x32_bf16 v[216:219], v[200:203], v[252:255], v[216:219]\n\t" \
  "s_waitcnt lgkmcnt(3)\n\t" "v_mfma_f32_16x16x32_bf16 v[220:223], v[196:199], v[32:35], v[220:223]\n\t" \
  "ds_read_b128 v[248:251], %[lr] offset:30976\n\t" "v_mfma_f32_16x16x32_bf16 v[224:227], v[200:203], v[32:35], v[224:227]\n\t" \
  "s_waitcnt lgkmcnt(3)\n\t" "v_mfma_f32_16x16x32_bf16 v[228:231], v[196:199], v[36:39], v[228:231]\n\t" \
  "ds_read_b128 v[252:255], %[lr] offset:63488\n\t" "v_mfma_f32_16x16x32_bf16 v[232:235], v[200:203], v[36:39], v[232:235]\n\t" \
  "s_waitcnt lgkmcnt(3)\n\t" "v_mfma_f32_16x16x32_bf16 v[236:239], v[196:199], v[40:43], v[236:239]\n\t" \
  "ds_read_b128 v[32:35], %[lr] offset:63744\n\t" "v_mfma_f32_16x16x32_bf16 v[240:243], v[200:203], v[40:43], v[240:243]\n\t" "Lya%=:\n\t" \
  "ds_read_b32 v36, %[sv] offset:4\n\t" "s_waitcnt lgkmcnt(0)\n\t" "v_readfirstlane_b32 s40, v36\n\t" "s_nop 3\n\t" "s_cmp_ge_i32 s40, %[ny]\n\t" \
  "s_cbranch_scc1 Lyb%=\n\t" "s_sleep 1\n\t" "s_branch Lya%=\n\t" "Lyb%=:\n\t" "s_waitcnt lgkmcnt(3)\n\t" \
  "v_mfma_f32_16x16x32_bf16 v[212:215], v[204:207], v[244:247], v[212:215]\n\t" "ds_read_b128 v[36:39], %[ma] offset:0\n\t" \
  "v_mfma_f32_16x16x32_bf16 v[216:219], v[208:211], v[244:247], v[216:219]\n\t" "s_waitcnt lgkmcnt(3)\n\t" \
  "v_mfma_f32_16x16x32_bf16 v[220:223], v[204:207], v[248:251], v[220:223]\n\t" "ds_read_b128 v[40:43], %[ma] offset:256\n\t" \
  "v_mfma_f32_16x16x32_bf16 v[224:227], v[208:211], v[248:251], v[224:227]\n\t" "s_waitcnt lgkmcnt(3)\n\t" \
  "v_mfma_f32_16x16x32_bf16 v[228:231], v[204:207], v[252:255], v[228:231]\n\t" "ds_read_b128 v[244:247], %[ma] offset:512\n\t" \
  "v_mfma_f32_16x16x32_bf16 v[232:235], v[208:211], v[252:255], v[232:235]\n\t" "s_waitcnt lgkmcnt(3)\n\t" \
  "v_mfma_f32_16x16x32_bf16 v[236:239], v[204:207], v[32:35], v[236:239]\n\t" "ds_read_b128 v[248:251], %[ma] offset:768\n\t" \
  "v_mfma_f32_16x16x32_bf16 v[240:243], v[208:211], v[32:35], v[240:243]\n\t" "s_waitcnt lgkmcnt(3)\n\t" \
  "v_mfma_f32_16x16x32_bf16 v[212:215], %[cg], v[36:39], v[212:215]\n\t" "v_mfma_f32_16x16x32_bf16 v[216:219], %[cu], v[36:39], v[216:219]\n\t" \
  "s_waitcnt lgkmcnt(2)\n\t" "v_mfma_f32_16x16x32_bf16 v[220:223], %[cg], v[40:43], v[220:223]\n\t" \
  "v_mfma_f32_16x16x32_bf16 v[224:227], %[cu], v[40:43], v[224:227]\n\t" "s_waitcnt lgkmcnt(1)\n\t" \
  "v_mfma_f32_16x16x32_bf16 v[228:231], %[cg], v[244:247], v[228:231]\n\t" "v_mfma_f32_16x16x32_bf16 v[232:235], %[cu], v[244:247], v[232:235]\n\t" \
  "s_waitcnt lgkmcnt(0)\n\t" "v_mfma_f32_16x16x32_bf16 v[236:239], %[cg], v[248:251], v[236:239]\n\t" \
  "v_mfma_f32_16x16x32_bf16 v[240:243], %[cu], v[248:251], v[240:243]\n\t" "s_nop 7\n\t" "s_nop 1\n\t" "v_mul_f32 v212, v212, v216\n\t" \
  "v_mul_f32 v213, v213, v217\n\t" "v_mul_f32 v214, v214, v218\n\t" "v_mul_f32 v215, v215, v219\n\t" "v_mul_f32 v220, v220, v224\n\t" \
  "v_mul_f32 v221, v221, v225\n\t" "v_mul_f32 v222, v222, v226\n\t" "v_mul_f32 v223, v223, v227\n\t" "v_cvt_pk_bf16_f32 v244, v212, v213\n\t" \
  "v_cvt_pk_bf16_f32 v246, v220, v221\n\t" "v_cvt_pk_bf16_f32 v245, v214, v215\n\t" "v_cvt_pk_bf16_f32 v247, v222, v223\n\t" "s_nop 1\n\t" \
  "v_permlane16_swap_b32 v244, v246\n\t" "v_permlane16_swap_b32 v245, v247\n\t" "global_store_dwordx4 %[vo], v[244:247], %[po0] sc1\n\t" \
  "v_mul_f32 v228, v228, v232\n\t" "v_mul_f32 v229, v229, v233\n\t" "v_mul_f32 v230, v230, v234\n\t" "v_mul_f32 v231, v231, v235\n\t" \
  "v_mul_f32 v236, v236, v240\n\t" "v_mul_f32 v237, v237, v241\n\t" "v_mul_f32 v238, v238, v242\n\t" "v_mul_f32 v239, v239, v243\n\t" \
  "v_cvt_pk_bf16_f32 v248, v228, v229\n\t" "v_cvt_pk_bf16_f32 v250, v236, v237\n\t" "v_cvt_pk_bf16_f32 v249, v230, v231\n\t" \
  "v_cvt_pk_bf16_f32 v251, v238, v239\n\t" "s_nop 1\n\t" "v_permlane16_swap_b32 v248, v250\n\t" "v_permlane16_swap_b32 v249, v251\n\t" \
  "global_store_dwordx4 %[vo], v[248:251], %[po1] sc1\n\t" "s_waitcnt vmcnt(0)\n\t" "s_mov_b64 exec, 1\n\t" "ds_inc_u32 %[sv], %[sv] offset:44\n\t" \
  "s_mov_b64 exec, -1\n\t" "s_branch Ldone%=\n\t" "Lseven%=:\n\t" "global_load_dwordx4 v[44:47], %[vo], s[42:43] offset:-4096 sc1\n\t" \
  "global_load_dwordx4 v[48:51], %[vo], s[42:43] offset:-3072 sc1\n\t" "Ldone%=:\n\t" "s_waitcnt vmcnt(0)\n\t"
#define GU_ASM_B \
  "s_waitcnt lgkmcnt(0)\n\t" "s_mov_b64 s[42:43], %[pd]\n\t" "s_cmp_eq_u32 %[wv], 7\n\t" "s_cbranch_scc1 Lseven%=\n\t" \
  "ds_read_b128 v[244:247], %[lr] offset:0\n\t" "ds_read_b128 v[248:251], %[lr] offset:256\n\t" "ds_read_b128 v[252:255], %[lr] offset:32768\n\t" \
  "ds_read_b128 v[32:35], %[lr] offset:33024\n\t" "s_waitcnt lgkmcnt(3)\n\t" "v_mfma_f32_16x16x32_bf16 v[212:215], v[84:87], v[244:247], 0\n\t" \
  "ds_read_b128 v[36:39], %[lr] offset:2048\n\t" "v_mfma_f32_16x16x32_bf16 v[216:219], v[88:91], v[244:247], 0\n\t" "s_waitcnt lgkmcnt(3)\n\t" \
  "v_mfma_f32_16x16x32_bf16 v[220:223], v[84:87], v[248:251], 0\n\t" "ds_read_b128 v[40:43], %[lr] offset:2304\n\t" \
  "v_mfma_f32_16x16x32_bf16 v[224:227], v[88:91], v[248:251], 0\n\t" "s_waitcnt lgkmcnt(3)\n\t" \
  "v_mfma_f32_16x16x32_bf16 v[228:231], v[84:87], v[252:255], 0\n\t" "ds_read_b128 v[244:247], %[lr] offset:34816\n\t" \
  "v_mfma_f32_16x16x32_bf16 v[232:235], v[88:91], v[252:255], 0\n\t" "s_waitcnt lgkmcnt(3)\n\t" \
  "v_mfma_f32_16x16x32_bf16 v[236:239], v[84:87], v[32:35], 0\n\t" "ds_read_b128 v[248:251], %[lr] offset:35072\n\t" \
  "v_mfma_f32_16x16x32_bf16 v[240:243], v[88:91], v[32:35], 0\n\t" "s_waitcnt lgkmcnt(3)\n\t" \
  "v_mfma_f32_16x16x32_bf16 v[212:215], v[92:95], v[36:39], v[212:215]\n\t" "ds_read_b128 v[252:255], %[lr] offset:4096\n\t" \
  "v_mfma_f32_16x16x32_bf16 v[216:219], v[96:99], v[36:39], v[216:219]\n\t" "global_load_dwordx4 v[84:87], %[vo], s[42:43] offset:-4096 sc1\n\t" \
  "s_waitcnt lgkmcnt(3)\n\t" "v_mfma_f32_16x16x32_bf16 v[220:223], v[92:95], v[40:43], v[220:223]\n\t" "ds_read_b128 v[32:35], %[lr] offset:4352\n\t" \
  "v_mfma_f32_16x16x32_bf16 v[224:227], v[96:99], v[40:43], v[224:227]\n\t" "s_waitcnt lgkmcnt(3)\n\t" \
  "v_mfma_f32_16x16x32_bf16 v[228:231], v[92:95], v[244:247], v[228:231]\n\t" "ds_read_b128 v[36:39], %[lr] offset:36864\n\t" \
  "v_mfma_f32_16x16x32_bf16 v[232:235], v[96:99], v[244:247], v[232:235]\n\t" "global_load_dwordx4 v[88:91], %[vo], s[42:43] offset:-3072 sc1\n\t" \
  "s_waitcnt lgkmcnt(3)\n\t" "v_mfma_f32_16x16x32_bf16 v[236:239], v[92:95], v[248:251], v[236:239]\n\t" \
  "ds_read_b128 v[40:43], %[lr] offset:37120\n\t" "v_mfma_f32_16x16x32_bf16 v[240:243], v[96:99], v[248:251], v[240:243]\n\t" \
  "s_waitcnt lgkmcnt(3)\n\t" "v_mfma_f32_16x16x32_bf16 v[212:215], v[100:103], v[252:255], v[212:215]\n\t" \
  "ds_read_b128 v[244:247], %[lr] offset:6144\n\t" "v_mfma_f32_16x16x32_bf16 v[216:219], v[104:107], v[252:255], v[216:219]\n\t" \
  "global_load_dwordx4 v[92:95], %[vo], s[42:43] offset:-2048 sc1\n\t" "s_waitcnt lgkmcnt(3)\n\t" \
  "v_mfma_f32_16x16x32_bf16 v[220:223], v[100:103], v[32:35], v[220:223]\n\t" "ds_read_b128 v[248:251], %[lr] offset:6400\n\t" \
  "v_mfma_f32_16x16x32_bf16 v[224:227], v[104:107], v[32:35], v[224:227]\n\t" "s_waitcnt lgkmcnt(3)\n\t" \
  "v_mfma_f32_16x16x32_bf16 v[228:231], v[100:103], v[36:39], v[228:231]\n\t" "ds_read_b128 v[252:255], %[lr] offset:38912\n\t" \
  "v_mfma_f32_16x16x32_bf16 v[232:235], v[104:107], v[36:39], v[232:235]\n\t" "global_load_dwordx4 v[96:99], %[vo], s[42:43] offset:-1024 sc1\n\t" \
  "s_waitcnt lgkmcnt(3)\n\t" "v_mfma_f32_16x16x32_bf16 v[236:239], v[100:103], v[40:43], v[236:239]\n\t" \
  "ds_read_b128 v[32:35], %[lr] offset:39168\n\t" "v_mfma_f32_16x16x32_bf16 v[240:243], v[104:107], v[40:43], v[240:243]\n\t" \
  "s_waitcnt lgkmcnt(3)\n\t" "v_mfma_f32_16x16x32_bf16 v[212:215], v[108:111], v[244:247], v[212:215]\n\t" \
  "ds_read_b128 v[36:39], %[lr] offset:8192\n\t" "v_mfma_f32_16x16x32_bf16 v[216:219], v[112:115], v[244:247], v[216:219]\n\t" \
  "global_load_dwordx4 v[100:103], %[vo], s[42:43] offset:0 sc1\n\t" "s_waitcnt lgkmcnt(3)\n\t" \
  "v_mfma_f32_16x16x32_bf16 v[220:223], v[108:111], v[248:251], v[220:223]\n\t" "ds_read_b128 v[40:43], %[lr] offset:8448\n\t" \
  "v_mfma_f32_16x16x32_bf16 v[224:227], v[112:115], v[248:251], v[224:227]\n\t" "s_waitcnt lgkmcnt(3)\n\t" \
  "v_mfma_f32_16x16x32_bf16 v[228:231], v[108:111], v[252:255], v[228:231]\n\t" "ds_read_b128 v[244:247], %[lr] offset:40960\n\t" \
  "v_mfma_f32_16x16x32_bf16 v[232:235], v[112:115], v[252:255], v[232:235]\n\t" "global_load_dwordx4 v[104:107], %[vo], s[42:43] offset:1024 sc1\n\t" \
  "s_waitcnt lgkmcnt(3)\n\t" "v_mfma_f32_16x16x32_bf16 v[236:239], v[108:111], v[32:35], v[236:239]\n\t" \
  "ds_read_b128 v[248:251], %[lr] offset:41216\n\t" "v_mfma_f32_16x16x32_bf16 v[240:243], v[112:115], v[32:35], v[240:243]\n\t" \
  "s_waitcnt lgkmcnt(3)\n\t" "v_mfma_f32_16x16x32_bf16 v[212:215], v[116:119], v[36:39], v[212:215]\n\t" \
  "ds_read_b128 v[252:255], %[lr] offset:10240\n\t" "v_mfma_f32_16x16x32_bf16 v[216:219], v[120:123], v[36:39], v[216:219]\n\t" \
  "global_load_dwordx4 v[108:111], %[vo], s[42:43] offset:2048 sc1\n\t" "s_waitcnt lgkmcnt(3)\n\t" \
  "v_mfma_f32_16x16x32_bf16 v[220:223], v[116:119], v[40:43], v[220:223]\n\t" "ds_read_b128 v[32:35], %[lr] offset:10496\n\t" \
  "v_mfma_f32_16x16x32_bf16 v[224:227], v[120:123], v[40:43], v[224:227]\n\t" "s_waitcnt lgkmcnt(3)\n\t" \
  "v_mfma_f32_16x16x32_bf16 v[228:231], v[116:119], v[244:247], v[228:231]\n\t" "ds_read_b128 v[36:39], %[lr] offset:43008\n\t" \
  "v_mfma_f32_16x16x32_bf16 v[232:235], v[120:123], v[244:247], v[232:235]\n\t" "global_load_dwordx4 v[112:115], %[vo], s[42:43] offset:3072 sc1\n\t" \
  "s_waitcnt lgkmcnt(3)\n\t" "v_mfma_f32_16x16x32_bf16 v[236:239], v[116:119], v[248:251], v[236:239]\n\t" \
  "ds_read_b128 v[40:43], %[lr] offset:43264\n\t" "v_mfma_f32_16x16x32_bf16 v[240:243], v[120:123], v[248:251], v[240:243]\n\t" \
  "s_waitcnt lgkmcnt(3)\n\t" "v_mfma_f32_16x16x32_bf16 v[212:215], v[124:127], v[252:255], v[212:215]\n\t" \
  "ds_read_b128 v[244:247], %[lr] offset:12288\n\t" "v_mfma_f32_16x16x32_bf16 v[216:219], v[128:131], v[252:255], v[216:219]\n\t" \
  "s_add_u32 s42, s42, 0x2000\n\t" "s_addc_u32 s43, s43, 0\n\t" "global_load_dwordx4 v[116:119], %[vo], s[42:43] offset:-4096 sc1\n\t" \
  "s_waitcnt lgkmcnt(3)\n\t" "v_mfma_f32_16x16x32_bf16 v[220:223], v[124:127], v[32:35], v[220:223]\n\t" \
  "ds_read_b128 v[248:251], %[lr] offset:12544\n\t" "v_mfma_f32_16x16x32_bf16 v[224:227], v[128:131], v[32:35], v[224:227]\n\t" \
  "s_waitcnt lgkmcnt(3)\n\t" "v_mfma_f32_16x16x32_bf16 v[228:231], v[124:127], v[36:39], v[228:231]\n\t" \
  "ds_read_b128 v[252:255], %[lr] offset:45056\n\t" "v_mfma_f32_16x16x32_bf16 v[232:235], v[128:131], v[36:39], v[232:235]\n\t" \
  "global_load_dwordx4 v[120:123], %[vo], s[42:43] offset:-3072 sc1\n\t" "s_waitcnt lgkmcnt(3)\n\t" \
  "v_mfma_f32_16x16x32_bf16 v[236:239], v[124:127], v[40:43], v[236:239]\n\t" "ds_read_b128 v[32:35], %[lr] offset:45312\n\t" \
  "v_mfma_f32_16x16x32_bf16 v[240:243], v[128:131], v[40:43], v[240:243]\n\t" "s_waitcnt lgkmcnt(3)\n\t" \
  "v_mfma_f32_16x16x32_bf16 v[212:215], v[132:135], v[244:247], v[212:215]\n\t" "ds_read_b128 v[36:39], %[lr] offset:14336\n\t" \
  "v_mfma_f32_16x16x32_bf16 v[216:219], v[136:139], v[244:247], v[216:219]\n\t" "global_load_dwordx4 v[124:127], %[vo], s[42:43] offset:-2048 sc1\n\t" \
  "s_waitcnt lgkmcnt(3)\n\t" "v_mfma_f32_16x16x32_bf16 v[220:223], v[132:135], v[248:251], v[220:223]\n\t" \
  "ds_read_b128 v[40:43], %[lr] offset:14592\n\t" "v_mfma_f32_16x16x32_bf16 v[224:227], v[136:139], v[248:251], v[224:227]\n\t" \
  "s_waitcnt lgkmcnt(3)\n\t" "v_mfma_f32_16x16x32_bf16 v[228:231], v[132:135], v[252:255], v[228:231]\n\t" \
  "ds_read_b128 v[244:247], %[lr] offset:47104\n\t" "v_mfma_f32_16x16x32_bf16 v[232:235], v[136:139], v[252:255], v[232:235]\n\t" \
  "global_load_dwordx4 v[128:131], %[vo], s[42:43] offset:-1024 sc1\n\t" "s_waitcnt lgkmcnt(3)\n\t" \
  "v_mfma_f32_16x16x32_bf16 v[236:239], v[132:135], v[32:35], v[236:239]\n\t" "ds_read_b128 v[248:251], %[lr] offset:47360\n\t" \
  "v_mfma_f32_16x16x32_bf16 v[240:243], v[136:139], v[32:35], v[240:243]\n\t" "s_waitcnt lgkmcnt(3)\n\t" \
  "v_mfma_f32_16x16x32_bf16 v[212:215], v[140:143], v[36:39], v[212:215]\n\t" "ds_read_b128 v[252:255], %[lr] offset:16384\n\t" \
  "v_mfma_f32_16x16x32_bf16 v[216:219], v[144:147], v[36:39], v[216:219]\n\t" "global_load_dwordx4 v[132:135], %[vo], s[42:43] offset:0 sc1\n\t" \
  "s_waitcnt lgkmcnt(3)\n\t" "v_mfma_f32_16x16x32_bf16 v[220:223], v[140:143], v[40:43], v[220:223]\n\t" \
  "ds_read_b128 v[32:35], %[lr] offset:16640\n\t" "v_mfma_f32_16x16x32_bf16 v[224:227], v[144:147], v[40:43], v[224:227]\n\t" \
  "s_waitcnt lgkmcnt(3)\n\t" "v_mfma_f32_16x16x32_bf16 v[228:231], v[140:143], v[244:247], v[228:231]\n\t" \
  "ds_read_b128 v[36:39], %[lr] offset:49152\n\t" "v_mfma_f32_16x16x32_bf16 v[232:235], v[144:147], v[244:247], v[232:235]\n\t" \
  "global_load_dwordx4 v[136:139], %[vo], s[42:43] offset:1024 sc1\n\t" "s_waitcnt lgkmcnt(3)\n\t" \
  "v_mfma_f32_16x16x32_bf16 v[236:239], v[140:143], v[248:251], v[236:239]\n\t" "ds_read_b128 v[40:43], %[lr] offset:49408\n\t" \
  "v_mfma_f32_16x16x32_bf16 v[240:243], v[144:147], v[248:251], v[240:243]\n\t" "s_waitcnt lgkmcnt(3)\n\t" \
  "v_mfma_f32_16x16x32_bf16 v[212:215], v[148:151], v[252:255], v[212:215]\n\t" "ds_read_b128 v[244:247], %[lr] offset:18432\n\t" \
  "v_mfma_f32_16x16x32_bf16 v[216:219], v[152:155], v[252:255], v[216:219]\n\t" "global_load_dwordx4 v[140:143], %[vo], s[42:43] offset:2048 sc1\n\t" \
  "s_waitcnt lgkmcnt(3)\n\t" "v_mfma_f32_16x16x32_bf16 v[220:223], v[148:151], v[32:35], v[220:223]\n\t" \
  "ds_read_b128 v[248:251], %[lr] offset:18688\n\t" "v_mfma_f32_16x16x32_bf16 v[224:227], v[152:155], v[32:35], v[224:227]\n\t" \
  "s_waitcnt lgkmcnt(3)\n\t" "v_mfma_f32_16x16x32_bf16 v[228:231], v[148:151], v[36:39], v[228:231]\n\t" \
  "ds_read_b128 v[252:255], %[lr] offset:51200\n\t" "v_mfma_f32_16x16x32_bf16 v[232:235], v[152:155], v[36:39], v[232:235]\n\t" \
  "global_load_dwordx4 v[144:147], %[vo], s[42:43] offset:3072 sc1\n\t" "s_waitcnt lgkmcnt(3)\n\t" \
  "v_mfma_f32_16x16x32_bf16 v[236:239], v[148:151], v[40:43], v[236:239]\n\t" "ds_read_b128 v[32:35], %[lr] offset:51456\n\t" \
  "v_mfma_f32_16x16x32_bf16 v[240:243], v[152:155], v[40:43], v[240:243]\n\t" "s_waitcnt lgkmcnt(3)\n\t" \
  "v_mfma_f32_16x16x32_bf16 v[212:215], v[156:159], v[244:247], v[212:215]\n\t" "ds_read_b128 v[36:39], %[lr] offset:20480\n\t" \
  "v_mfma_f32_16x16x32_bf16 v[216:219], v[160:163], v[244:247], v[216:219]\n\t" "s_add_u32 s42, s42, 0x2000\n\t" "s_addc_u32 s43, s43, 0\n\t" \
  "global_load_dwordx4 v[148:151], %[vo], s[42:43] offset:-4096 sc1\n\t" "s_waitcnt lgkmcnt(3)\n\t" \
  "v_mfma_f32_16x16x32_bf16 v[220:223], v[156:159], v[248:251], v[220:223]\n\t" "ds_read_b128 v[40:43], %[lr] offset:20736\n\t" \
  "v_mfma_f32_16x16x32_bf16 v[224:227], v[160:163], v[248:251], v[224:227]\n\t" "s_waitcnt lgkmcnt(3)\n\t" \
  "v_mfma_f32_16x16x32_bf16 v[228:231], v[156:159], v[252:255], v[228:231]\n\t" "ds_read_b128 v[244:247], %[lr] offset:53248\n\t" \
  "v_mfma_f32_16x16x32_bf16 v[232:235], v[160:163], v[252:255], v[232:235]\n\t" "global_load_dwordx4 v[152:155], %[vo], s[42:43] offset:-3072 sc1\n\t" \
  "s_waitcnt lgkmcnt(3)\n\t" "v_mfma_f32_16x16x32_bf16 v[236:239], v[156:159], v[32:35], v[236:239]\n\t" \
  "ds_read_b128 v[248:251], %[lr] offset:53504\n\t" "v_mfma_f32_16x16x32_bf16 v[240:243], v[160:163], v[32:35], v[240:243]\n\t" \
  "s_waitcnt lgkmcnt(3)\n\t" "v_mfma_f32_16x16x32_bf16 v[212:215], v[164:167], v[36:39], v[212:215]\n\t" \
  "ds_read_b128 v[252:255], %[lr] offset:22528\n\t" "v_mfma_f32_16x16x32_bf16 v[216:219], v[168:171], v[36:39], v[216:219]\n\t" \
  "s_waitcnt lgkmcnt(3)\n\t" "v_mfma_f32_16x16x32_bf16 v[220:223], v[164:167], v[40:43], v[220:223]\n\t" \
  "ds_read_b128 v[32:35], %[lr] offset:22784\n\t" "v_mfma_f32_16x16x32_bf16 v[224:227], v[168:171], v[40:43], v[224:227]\n\t" \
  "s_waitcnt lgkmcnt(3)\n\t" "v_mfma_f32_16x16x32_bf16 v[228:231], v[164:167], v[244:247], v[228:231]\n\t" \
  "ds_read_b128 v[36:39], %[lr] offset:55296\n\t" "v_mfma_f32_16x16x32_bf16 v[232:235], v[168:171], v[244:247], v[232:235]\n\t" \
  "s_waitcnt lgkmcnt(3)\n\t" "v_mfma_f32_16x16x32_bf16 v[236:239], v[164:167], v[248:251], v[236:239]\n\t" \
  "ds_read_b128 v[40:43], %[lr] offset:55552\n\t" "v_mfma_f32_16x16x32_bf16 v[240:243], v[168:171], v[248:251], v[240:243]\n\t" \
  "s_waitcnt lgkmcnt(3)\n\t" "v_mfma_f32_16x16x32_bf16 v[212:215], v[172:175], v[252:255], v[212:215]\n\t" \
  "ds_read_b128 v[244:247], %[lr] offset:24576\n\t" "v_mfma_f32_16x16x32_bf16 v[216:219], v[176:179], v[252:255], v[216:219]\n\t" \
  "s_waitcnt lgkmcnt(3)\n\t" "v_mfma_f32_16x16x32_bf16 v[220:223], v[172:175], v[32:35], v[220:223]\n\t" \
  "ds_read_b128 v[248:251], %[lr] offset:24832\n\t" "v_mfma_f32_16x16x32_bf16 v[224:227], v[176:179], v[32:35], v[224:227]\n\t" \
  "s_waitcnt lgkmcnt(3)\n\t" "v_mfma_f32_16x16x32_bf16 v[228:231], v[172:175], v[36:39], v[228:231]\n\t" \
  "ds_read_b128 v[252:255], %[lr] offset:57344\n\t" "v_mfma_f32_16x16x32_bf16 v[232:235], v[176:179], v[36:39], v[232:235]\n\t" \
  "s_waitcnt lgkmcnt(3)\n\t" "v_mfma_f32_16x16x32_bf16 v[236:239], v[172:175], v[40:43], v[236:239]\n\t" \
  "ds_read_b128 v[32:35], %[lr] offset:57600\n\t" "v_mfma_f32_16x16x32_bf16 v[240:243], v[176:179], v[40:43], v[240:243]\n\t" \
  "s_waitcnt lgkmcnt(3)\n\t" "v_mfma_f32_16x16x32_bf16 v[212:215], v[180:183], v[244:247], v[212:215]\n\t" \
  "ds_read_b128 v[36:39], %[lr] offset:26624\n\t" "v_mfma_f32_16x16x32_bf16 v[216:219], v[184:187], v[244:247], v[216:219]\n\t" \
  "s_waitcnt lgkmcnt(3)\n\t" "v_mfma_f32_16x16x32_bf16 v[220:223], v[180:183], v[248:251], v[220:223]\n\t" \
  "ds_read_b128 v[40:43], %[lr] offset:26880\n\t" "v_mfma_f32_16x16x32_bf16 v[224:227], v[184:187], v[248:251], v[224:227]\n\t" \
  "s_waitcnt lgkmcnt(3)\n\t" "v_mfma_f32_16x16x32_bf16 v[228:231], v[180:183], v[252:255], v[228:231]\n\t" \
  "ds_read_b128 v[244:247], %[lr] offset:59392\n\t" "v_mfma_f32_16x16x32_bf16 v[232:235], v[184:187], v[252:255], v[232:235]\n\t" \
  "s_waitcnt lgkmcnt(3)\n\t" "v_mfma_f32_16x16x32_bf16 v[236:239], v[180:183], v[32:35], v[236:239]\n\t" \
  "ds_read_b128 v[248:251], %[lr] offset:59648\n\t" "v_mfma_f32_16x16x32_bf16 v[240:243], v[184:187], v[32:35], v[240:243]\n\t" \
  "s_waitcnt lgkmcnt(3)\n\t" "v_mfma_f32_16x16x32_bf16 v[212:215], v[188:191], v[36:39], v[212:215]\n\t" \
  "ds_read_b128 v[252:255], %[lr] offset:28672\n\t" "v_mfma_f32_16x16x32_bf16 v[216:219], v[192:195], v[36:39], v[216:219]\n\t" \
  "s_waitcnt lgkmcnt(3)\n\t" "v_mfma_f32_16x16x32_bf16 v[220:223], v[188:191], v[40:43], v[220:223]\n\t" \
  "ds_read_b128 v[32:35], %[lr] offset:28928\n\t" "v_mfma_f32_16x16x32_bf16 v[224:227], v[192:195], v[40:43], v[224:227]\n\t" \
  "s_waitcnt lgkmcnt(3)\n\t" "v_mfma_f32_16x16x32_bf16 v[228:231], v[188:191], v[244:247], v[228:231]\n\t" \
  "ds_read_b128 v[36:39], %[lr] offset:61440\n\t" "v_mfma_f32_16x16x32_bf16 v[232:235], v[192:195], v[244:247], v[232:235]\n\t" \
  "s_waitcnt lgkmcnt(3)\n\t" "v_mfma_f32_16x16x32_bf16 v[236:239], v[188:191], v[248:251], v[236:239]\n\t" \
  "ds_read_b128 v[40:43], %[lr] offset:61696\n\t" "v_mfma_f32_16x16x32_bf16 v[240:243], v[192:195], v[248:251], v[240:243]\n\t" \
  "s_waitcnt lgkmcnt(3)\n\t" "v_mfma_f32_16x16x32_bf16 v[212:215], v[196:199], v[252:255], v[212:215]\n\t" \
  "ds_read_b128 v[244:247], %[lr] offset:30720\n\t" "v_mfma_f32_16x16x32_bf16 v[216:219], v[200:203], v[252:255], v[216:219]\n\t" \
  "s_waitcnt lgkmcnt(3)\n\t" "v_mfma_f32_16x16x32_bf16 v[220:223], v[196:199], v[32:35], v[220:223]\n\t" \
  "ds_read_b128 v[248:251], %[lr] offset:30976\n\t" "v_mfma_f32_16x16x32_bf16 v[224:227], v[200:203], v[32:35], v[224:227]\n\t" \
  "s_waitcnt lgkmcnt(3)\n\t" "v_mfma_f32_16x16x32_bf16 v[228:231], v[196:199], v[36:39], v[228:231]\n\t" \
  "ds_read_b128 v[252:255], %[lr] offset:63488\n\t" "v_mfma_f32_16x16x32_bf16 v[232:235], v[200:203], v[36:39], v[232:235]\n\t" \
  "s_waitcnt lgkmcnt(3)\n\t" "v_mfma_f32_16x16x32_bf16 v[236:239], v[196:199], v[40:43], v[236:239]\n\t" \
  "ds_read_b128 v[32:35], %[lr] offset:63744\n\t" "v_mfma_f32_16x16x32_bf16 v[240:243], v[200:203], v[40:43], v[240:243]\n\t" \
  "s_waitcnt lgkmcnt(3)\n\t" "v_mfma_f32_16x16x32_bf16 v[212:215], v[204:207], v[244:247], v[212:215]\n\t" "ds_read_b128 v[36:39], %[ma] offset:0\n\t" \
  "v_mfma_f32_16x16x32_bf16 v[216:219], v[208:211], v[244:247], v[216:219]\n\t" "s_waitcnt lgkmcnt(3)\n\t" \
  "v_mfma_f32_16x16x32_bf16 v[220:223], v[204:207], v[248:251], v[220:223]\n\t" "ds_read_b128 v[40:43], %[ma] offset:256\n\t" \
  "v_mfma_f32_16x16x32_bf16 v[224:227], v[208:211], v[248:251], v[224:227]\n\t" "s_waitcnt lgkmcnt(3)\n\t" \
  "v_mfma_f32_16x16x32_bf16 v[228:231], v[204:207], v[252:255], v[228:231]\n\t" "ds_read_b128 v[244:247], %[ma] offset:512\n\t" \
  "v_mfma_f32_16x16x32_bf16 v[232:235], v[208:211], v[252:255], v[232:235]\n\t" "s_waitcnt lgkmcnt(3)\n\t" \
  "v_mfma_f32_16x16x32_bf16 v[236:239], v[204:207], v[32:35], v[236:239]\n\t" "ds_read_b128 v[248:251], %[ma] offset:768\n\t" \
  "v_mfma_f32_16x16x32_bf16 v[240:243], v[208:211], v[32:35], v[240:243]\n\t" "s_waitcnt lgkmcnt(3)\n\t" \
  "v_mfma_f32_16x16x32_bf16 v[212:215], %[cg], v[36:39], v[212:215]\n\t" "v_mfma_f32_16x16x32_bf16 v[216:219], %[cu], v[36:39], v[216:219]\n\t" \
  "s_waitcnt lgkmcnt(2)\n\t" "v_mfma_f32_16x16x32_bf16 v[220:223], %[cg], v[40:43], v[220:223]\n\t" \
  "v_mfma_f32_16x16x32_bf16 v[224:227], %[cu], v[40:43], v[224:227]\n\t" "s_waitcnt lgkmcnt(1)\n\t" \
  "v_mfma_f32_16x16x32_bf16 v[228:231], %[cg], v[244:247], v[228:231]\n\t" "v_mfma_f32_16x16x32_bf16 v[232:235], %[cu], v[244:247], v[232:235]\n\t" \
  "s_waitcnt lgkmcnt(0)\n\t" "v_mfma_f32_16x16x32_bf16 v[236:239], %[cg], v[248:251], v[236:239]\n\t" \
  "v_mfma_f32_16x16x32_bf16 v[240:243], %[cu], v[248:251], v[240:243]\n\t" "s_nop 7\n\t" "s_nop 1\n\t" "v_mul_f32 v212, v212, v216\n\t" \
  "v_mul_f32 v213, v213, v217\n\t" "v_mul_f32 v214, v214, v218\n\t" "v_mul_f32 v215, v215, v219\n\t" "v_mul_f32 v220, v220, v224\n\t" \
  "v_mul_f32 v221, v221, v225\n\t" "v_mul_f32 v222, v222, v226\n\t" "v_mul_f32 v223, v223, v227\n\t" "v_cvt_pk_bf16_f32 v244, v212, v213\n\t" \
  "v_cvt_pk_bf16_f32 v246, v220, v221\n\t" "v_cvt_pk_bf16_f32 v245, v214, v215\n\t" "v_cvt_pk_bf16_f32 v247, v222, v223\n\t" "s_nop 1\n\t" \
  "v_permlane16_swap_b32 v244, v246\n\t" "v_permlane16_swap_b32 v245, v247\n\t" "global_store_dwordx4 %[vo], v[244:247], %[po0] sc1\n\t" \
  "v_mul_f32 v228, v228, v232\n\t" "v_mul_f32 v229, v229, v233\n\t" "v_mul_f32 v230, v230, v234\n\t" "v_mul_f32 v231, v231, v235\n\t" \
  "v_mul_f32 v236, v236, v240\n\t" "v_mul_f32 v237, v237, v241\n\t" "v_mul_f32 v238, v238, v242\n\t" "v_mul_f32 v239, v239, v243\n\t" \
  "v_cvt_pk_bf16_f32 v248, v228, v229\n\t" "v_cvt_pk_bf16_f32 v250, v236, v237\n\t" "v_cvt_pk_bf16_f32 v249, v230, v231\n\t" \
  "v_cvt_pk_bf16_f32 v251, v238, v239\n\t" "s_nop 1\n\t" "v_permlane16_swap_b32 v248, v250\n\t" "v_permlane16_swap_b32 v249, v251\n\t" \
  "global_store_dwordx4 %[vo], v[248:251], %[po1] sc1\n\t" "s_waitcnt vmcnt(0)\n\t" "s_mov_b64 exec, 1\n\t" "ds_inc_u32 %[sv], %[sv] offset:60\n\t" \
  "s_mov_b64 exec, -1\n\t" "s_branch Ldone%=\n\t" "Lseven%=:\n\t" "Ldone%=:\n\t" "s_waitcnt vmcnt(0)\n\t"
#define GU_CLOB "vcc", "s40", "s42", "s43", "v32", "v33", "v34", "v35", "v36", "v37", "v38", "v39", "v40", "v41", "v42", "v43", ATT_CLOB
template <int TT>
DEVI void gu_pass(const Ctx& c, int layer, DnRegs& R, u32x4 cgf, u32x4 cuf) {
  CTX_IDS();
  int lr = smb + TT * 65536 + (lane >> 5) * 1024 + ((lane >> 4) & 1) * 512 + (lane & 15) * 16;
  int ma = smb + GU_MF + TT * 1024 + (lane & 15) * 16, sv = smb + (TT ? S_V : S_X + 4);
  unsigned vo = l16;
  const char* po = ws + gb + G_H + (((r >> 4) * 4 + TT) * 112 + (r & 15) * 7 + w) * 1024;
  const char* pd = ws + WS_WDT + (nt8 * 8 + w) * 28672 + 4096;
  PIN(lr); PIN(ma); PIN(vo); PIN(sv);
  lds_wait<1>(sm + S_X + TT * 12, 4 * layer + 4);
  STAMP(15 + 10 * TT);
  if constexpr (TT == 0)
    asm volatile(GU_ASM_A
                 : "=&{v[44:51]}"(R.d2)
                 : "{v[84:115]}"(R.wp[0]), "{v[116:147]}"(R.wp[1]), "{v[148:179]}"(R.wp[2]), "{v[180:211]}"(R.wp[3]),
                   [lr] "v"(lr), [ma] "v"(ma), [vo] "v"(vo), [cg] "v"(cgf), [cu] "v"(cuf), [pd] "s"(pd + 8192), [po0] "s"(po),
                   [po1] "s"(po + 224 * 1024), [sv] "v"(sv), [nx] "s"(4 * layer + 4), [ny] "s"(layer + 1), [wv] "s"(w), "{v[68:83]}"(R.d0), "{v[52:67]}"(R.d1), "{v[28:31]}"(R.dx)
                 : GU_CLOB);
  else
    asm volatile(GU_ASM_B
                 : "+{v[84:115]}"(R.wp[0]), "+{v[116:147]}"(R.wp[1]), "+{v[148:179]}"(R.wp[2]), "+{v[180:211]}"(R.wp[3])
                 : [lr] "v"(lr), [ma] "v"(ma), [vo] "v"(vo), [cg] "v"(cgf), [cu] "v"(cuf), [pd] "s"(pd + 10240), [po0] "s"(po),
                   [po1] "s"(po + 224 * 1024), [sv] "v"(sv), [wv] "s"(w), "{v[68:83]}"(R.d0), "{v[52:67]}"(R.d1), "{v[44:51]}"(R.d2), "{v[28:31]}"(R.dx)
                 : GU_CLOB);
  if ((BACK & 4) && w < 7) { glds(vo, po, smb + WARM_PAD); glds(vo, po + 224 * 1024, smb + WARM_PAD); }
  if (TT == 0 && w == 7) { lds_inc(sm + S_X + 12); h_flag(c, layer, 0); __builtin_amdgcn_s_sleep(H_NAP); h_poll(c, layer, 0); }
  STAMP(16 + 10 * TT);
}

DEVI void issue_wkv(const Ctx& c, const char* wkv) {
  CTX_IDS();
  glds4(l16, wkv + (w & 3) * 4096, smb + 131072 + (w & 3) * 4096);
}
DEVI void issue_wq(const Ctx& c, const char* wq) {
  CTX_IDS();
  const unsigned vo = (lane >> 3) * 1024 + (((lane & 7) ^ ((lane >> 2) & 4)) << 4);
#pragma unroll 4
  for (int i = 0; i < 16; i++) glds(vo, wq + (i * 4 + w) * 8192 + nt8 * 128, smb + QKV_W + (i * 4 + w) * 1024);
}
DEVI void prep_load(int su, int lane, const u16* wg, const u16* wu, const u16* wd, const u16* wo, u32x4 (&pv)[8]) {
  const char* src; int ld;
  if (su < 448) { const int v = su < 224 ? su : su - 224; ld = F_ * 2; src = (const char*)(su < 224 ? wg : wu) + (v & 7) * 64 * ld + (v >> 3) * 128; }
  else { const int v = su < 672 ? su - 448 : su - 672; ld = D_ * 2; src = (const char*)(su < 672 ? wd : wo) + (v >> 3) * 64 * ld + (v & 7) * 128; }
  const unsigned vo = (lane >> 3) * ld + (lane & 7) * 16;
#pragma unroll
  for (int i = 0; i < 8; i++) asm volatile("global_load_dwordx4 %0, %1, %2" : "=v"(pv[i]) : "v"(vo), "s"(src + 8 * i * ld) : "memory");
}
DEVI void prep_store(const Ctx& c, int su, int base, u32x4 (&pv)[8]) {
  CTX_IDS();
  const int hh = lane >> 5, g1 = (lane >> 4) & 1, q = (lane & 15) >> 2, p = lane & 3;
  const bool isd = su >= 448 && su < 672;
  const int col = isd ? 16 * g1 + 8 * (p & 1) + 4 * (p >> 1) : 4 * p;
  const int ra = ((isd ? 8 * hh : 8 * (lane >> 4)) + q) * 128 + (((col >> 3) ^ ((q & 2) << 1)) << 4) + (col & 7) * 2;
  const int wa = base + (lane >> 3) * 128 + (((lane & 7) ^ ((lane >> 2) & 4)) << 4);
  unsigned d0, si, sh;
  if (isd) { const int v = su - 448; d0 = WS_WDT + ((v & 7) * 112 + (v >> 3) * 4) * 2048; si = 1024; sh = 4096; }
  else if (su < 448) { const int uu = su >= 224, v = su - 224 * uu; d0 = WS_WGUT + (((v >> 3) * 64 + (v & 7) * 2) * 2 + uu) * 1024; si = 32768; sh = 2048; }
  else { const int v = su - 672; d0 = WS_WOT + ((v & 7) * 64 + (v >> 3) * 2) * 1024; si = 16384; sh = 1024; }
  asm volatile("s_waitcnt vmcnt(0)" : "+v"(pv[0]), "+v"(pv[1]), "+v"(pv[2]), "+v"(pv[3]), "+v"(pv[4]), "+v"(pv[5]), "+v"(pv[6]), "+v"(pv[7]) :: "memory");
#pragma unroll
  for (int hf = 0; hf < 2; hf++) {
#pragma unroll
    for (int i = 0; i < 4; i++) *(LAS u32x4*)(sm + wa + i * 1024) = pv[hf * 4 + i];
    asm volatile("" ::: "memory");
#pragma unroll
    for (int i = 0; i < 4; i++) {
      const int a = base + (isd ? (ra ^ ((i & 1) * 64)) + (i >> 1) * 2048 : (ra ^ (i * 32)));
      pv[hf * 4 + i] = lds_tr(sm, a, a + 512);
    }
    asm volatile("" ::: "memory");
  }
  lds_inc(sm + S_PR + 4);
  lds_wait<2>(sm + S_PR, P_AT);
#pragma unroll
  for (int k = 0; k < 8; k++) st16(rs, l16, d0 + (k & 3) * si + (k >> 2) * sh, pv[k]);
}

template <int layer>
DEVI void phase_qkv(const Ctx& c, u32x4 (&pv)[8]) {
  CTX_IDS();
  constexpr unsigned KB = WS_KV + layer * KV_SIZE;
  const int hh = lane >> 5, g1 = (lane >> 4) & 1, q = (lane & 15) >> 2, p = lane & 3;
  const u32x4 ones = {0x3f803f80u, 0x3f803f80u, 0x3f803f80u, 0x3f803f80u};
  if (layer) { bar(); if (w == 0) set_flag1(ws + (gb + G_FDN + r * FL), opaque_t(c.tag)); }
  if (layer) {
    __builtin_amdgcn_s_sleep(D_NAP);
    poll(ws + gb + G_FDN + ((r & ~7) + w) * FL, fresh(0), c.tag);
    STAMP(1);
    glds4(l16, ws + gb + G_X2 + (m4 * 64 + 4 * w) * 1024, smb + QKV_X + 4 * w * 1024);
    glds4(l16, ws + gb + G_X2 + (m4 * 64 + 32 + 4 * w) * 1024, smb + QKV_X + 32768 + 4 * w * 1024);
  }
  if (w < 4) {
    const int tt = w & 1, ct = w >> 1, tok = 32 * tt + (lane & 31);
    const int wcol = 32 * ct + 16 * g1 + 8 * (p & 1) + 4 * (p >> 1);
    int wa = QKV_W + (8 * hh + q) * 128 + (((wcol >> 3) ^ ((q & 2) << 1)) << 4) + (wcol & 7) * 2;
    int qo = QKV_Q + w * 2048 + l16, sa = QKV_ST + 512 + tok * 8, ca = QKV_ST + (32 * ct + 8 * hh) * 4;
    int xa[8];
#pragma unroll
    for (int i = 0; i < 8; i++) { xa[i] = QKV_X + (layer ? tt * 32768 + l16 : tok * 1024 + (((i * 2 + hh) ^ (tok & 15)) << 4)); if (!layer || !i) PIN(xa[i]); }
    PIN(wa); PIN(qo); PIN(sa); PIN(ca);
    if (layer) { WAIT_VM(0); bar(); issue_wq(c, karg(A_WQ)); WAIT_VM(0); }
    else WAIT_VM(0);
    lds_inc(sm + S_WQ);
    lds_wait<1>(sm + S_WQ, 4 * layer + 8);
    u32x4 wf[32];
#pragma unroll
    for (int ks = 0; ks < 32; ks++) wf[ks] = lds_tr(sm, wa + ks * 2048, wa + ks * 2048 + 512);
    {
      f32x16 cs = {};
      if (tt) {
#pragma unroll
        for (int i = 16; i < 32; i++) cs = mfma32(wf[i], ones, cs);
      } else {
#pragma unroll
        for (int i = 0; i < 16; i++) cs = mfma32(wf[i], ones, cs);
      }
      if ((lane & 31) == 0) {
#pragma unroll
        for (int i = 0; i < 4; i++)
          *(LAS f32x4*)(sm + ca + tt * 256 + (16 * (i >> 1) + 4 * (i & 1)) * 4) = f32x4{cs[4 * i], cs[4 * i + 1], cs[4 * i + 2], cs[4 * i + 3]};
      }
      lds_inc(sm + S_ST);
    }
    SCHED();
    f32x16 acc = {};
    {
      u32x4 xf[32];
#pragma unroll
      for (int ks = 0; ks < 36; ks++) {
        if (ks < 32) xf[ks] = lds_rd(sm, layer ? xa[0] + ks * 1024 : xa[ks & 7] + (ks >> 3) * 256);
        if (ks >= 4) acc = mfma32(wf[ks - 4], xf[ks - 4], acc);
        SCHED();
      }
    }
    PIN(acc);
    lds_wait<1>(sm + S_ST, 8 * layer + 8);
    const f32x2 st = *(const LAS f32x2*)(sm + sa);
    const float sc = QSCALE * st[1], nm = -st[0] * sc;
    u32x4 o[2];
#pragma unroll
    for (int i = 0; i < 4; i++) {
      const f32x4 cs = lds_rdf(sm, ca + (16 * (i >> 1) + 4 * (i & 1)) * 4) + lds_rdf(sm, ca + 256 + (16 * (i >> 1) + 4 * (i & 1)) * 4);
      const f32x4 av = {acc[4 * i], acc[4 * i + 1], acc[4 * i + 2], acc[4 * i + 3]};
      const f32x4 v = av * sc + cs * nm;
      o[i >> 1][(i & 1) * 2] = pack2(v[0], v[1]); o[i >> 1][(i & 1) * 2 + 1] = pack2(v[2], v[3]);
    }
    *(LAS u32x4*)(sm + qo) = o[0];
    *(LAS u32x4*)(sm + qo + 1024) = o[1];
  } else {
    const bool isv = nt8 & 1, kvw = (w - 4) == (nt8 >> 1);
    const int t16 = 16 * (w - 4) + (lane & 15), kg = lane >> 4;
    int ka = (layer ? 131072 : 0) + (8 * kg + q) * 32 + p * 8;
    int xa[4];
#pragma unroll
    for (int i = 0; i < 4; i++) {
      xa[i] = QKV_X + (layer ? (t16 >> 5) * 32768 + (kg >> 1) * 1024 + ((kg & 1) * 32 + (t16 & 31)) * 16 : t16 * 1024 + (((i * 4 + kg) ^ (t16 & 15)) << 4));
      if (!layer || !i) PIN(xa[i]);
    }
    const int sub = (nt8 >> 1) & 1;
    unsigned vo = isv ? (g1 ? 1536 + sub * 544 + (lane & 1) * 16 : 1024 + sub * 544 + hh * 256 + swap23(lane & 15) * 16)
                      : (hh * 32 + sub * 16 + (lane & 15)) * 16;
    vo += KB + g * KVG + (m4 >> 1) * KVH + ((m4 & 1) * 2 + (nt8 >> 2)) * KVT;
    PIN(ka); PIN(vo);
    const int su = min(vb * 3 + ((w - (nt8 >> 1) + 3) & 3), 735);
    if (!layer) {
      if (kvw) WAIT_VM(1); else WAIT_VM(8);
      lds_inc(sm + S_WQ);
    }
    u32x4 wf[16];
    const float fz = __builtin_bit_cast(float, fresh(0));
    f32x4 kv = {fz, fz, fz, fz}, cs = kv, gr = {};
    if (kvw) {
#pragma unroll
      for (int i = 0; i < 16; i++) wf[i] = lds_tr(sm, ka + i * 1024, ka + i * 1024 + 128);
      if (isv) {
#pragma unroll
        for (int i = 0; i < 16; i++) cs = mfma16(ones, wf[i], cs);
      } else {
#pragma unroll
        for (int i = 0; i < 16; i++) cs = mfma16(wf[i], ones, cs);
      }
      __builtin_amdgcn_s_setprio(1);
      if (!layer) lds_inc(sm + S_PR);
    }
    SCHED();
    if (layer) { WAIT_VM(0); bar(); }
    STAMP(2);
    f32x4 sx = {};
    u32x4 xf[16];
#pragma unroll
    for (int i = 0; i < 16; i++) xf[i] = lds_rd(sm, layer ? xa[0] + i * 2048 : xa[i & 3] + (i >> 2) * 256);
#pragma unroll
    for (int i = 0; i < 16; i++) { gr = mfma16(xf[i], xf[i], gr); sx = mfma16(ones, xf[i], sx); }
    if (kvw) {
      if (isv) {
#pragma unroll
        for (int i = 0; i < 16; i++) kv = mfma16(xf[i], wf[i], kv);
      } else {
#pragma unroll
        for (int i = 0; i < 16; i++) kv = mfma16(wf[i], xf[i], kv);
      }
    }
    float invd = 1.f / D_;
    asm volatile("" : "+s"(invd));
    const float mean = sx[0] * invd;
    const float dg = (p & 2) ? ((p & 1) ? gr[3] : gr[2]) : ((p & 1) ? gr[1] : gr[0]);
    const float ss = red32<OpAdd>(red16<OpAdd>(kg == q ? dg : 0.f));
    const float rstd = __builtin_amdgcn_rsqf(fmaxf(ss * invd - mean * mean, 0.f) + LN_EPS);
    if (lane < 16) *(LAS f32x2*)(sm + QKV_ST + 512 + t16 * 8) = f32x2{mean, rstd};
    lds_inc(sm + S_ST);
    STAMP(3);
    if (kvw) {
      f32x4 v;
      if (isv) {
        asm volatile("s_waitcnt lgkmcnt(0)" ::: "memory");
        const f32x4 s0 = lds_rdf(sm, QKV_ST + 512 + (16 * (w - 4) + 4 * kg) * 8), s1 = lds_rdf(sm, QKV_ST + 528 + (16 * (w - 4) + 4 * kg) * 8);
        v = (kv - f32x4{s0[0], s0[2], s1[0], s1[2]} * cs) * f32x4{s0[1], s0[3], s1[1], s1[3]};
      } else v = (kv - mean * cs) * rstd;
      const unsigned p0 = pack2(v[0], v[1]), p1 = pack2(v[2], v[3]);
      const auto e0 = __builtin_amdgcn_permlane16_swap(p0, p0, false, false);
      const auto e1 = __builtin_amdgcn_permlane16_swap(p1, p1, false, false);
      u32x4 o = {e0[0], e1[0], e0[1], e1[1]};
      if (isv) {
        if (g1) { const unsigned k = (lane & 1) ? 0u : 0x3f803f80u; o = u32x4{k, k, k, k}; }
        if (!g1 || lane < 18) st16(rs, vo, 0, o);
      } else {
        if (!g1) st16(rs, vo, 0, o);
        float kn = dot2(p0, p0, dot2(p1, p1, 0.f));
        kn = red_row<OpMax>(red32<OpAdd>(red16<OpAdd>(kn)));
        if (lane == 0) __hip_atomic_store((GAS float*)(ws + KB + KV_MAX) + 4 * m64 + (nt8 >> 1), kn, __ATOMIC_RELAXED, __HIP_MEMORY_SCOPE_AGENT);
      }
      STAMP(4);
      WAIT_VM(0);
      if (lane < 8) set_flag(ws, WS_FKV + ((layer * 8 + lane) * 256 + vb) * FL, c.tag);
      if (BACK & 16) glds(vo, ws, smb + WARM_PAD);
      STAMP(5);
      if (!layer) lds_inc(sm + S_PR);
      __builtin_amdgcn_s_sleep(K_NAP);
      poll(ws + WS_FKV + ((layer * 8 + g) * 256 + g * 32) * FL, (lane & 31) * FL, c.tag);
      STAMP(6);
      if (!layer) lds_wait<1>(sm + S_PR + 4, 3);
#pragma unroll
      for (int i = 0; i < 4; i++) glds4(l16, ws + KB + g * KVG + i * 4096, smb + i * 4096);
      glds(l16, ws + KB + g * KVG + 16384, smb + 16384);
      if (!layer) lds_inc(sm + S_PR);
      WAIT_VM(0);
      __builtin_amdgcn_s_setprio(0);
    } else if (!layer) {
      lds_wait<1>(sm + S_PR, 1);
      prep_store(c, su, (w - 4) * 4096, pv);
    }
  }
}

__global__ __launch_bounds__(512) void k_mega(const u16* xin, const u16* wq, const u16* wk, const u16* wv, const u16* wo,
                                              const u16* wg, const u16* wu, const u16* wd, char* ws, u16* out) {
  extern __shared__ __attribute__((aligned(16))) char smem[];
  LAS char* sm = (LAS char*)smem;
  const int w_ = __builtin_amdgcn_readfirstlane(threadIdx.x >> 6);
  const int g_ = blockIdx.x & 7, r_ = blockIdx.x >> 3;
  const rsrc_t rs = __builtin_amdgcn_make_buffer_rsrc(ws, 0, WS_END, 0x00020000);
  int layer = 0;
  STAMP(0);
  Ctx cx = {ws, sm, rs, 0, w_, g_, r_};
  u64 tag;
  {
    PHASE_IDS();
    if (w == 0) *(LAS int*)(sm + SY + lane * 4) = 0;
    lds_bar();
    asm volatile("s_load_dwordx2 %0, %1, 0x0" : "=&s"(tag) : "s"(ws + WS_EPOCH), "s"(xin), "s"(wq), "s"(wk), "s"(wv), "s"(wo), "s"(wg), "s"(wu), "s"(wd) : "memory");
    u32x4 pv[8];
    if (w >= 4) {
      const bool kvw = w - 4 == (nt8 >> 1);
      if (kvw) __builtin_amdgcn_s_setprio(2); else __builtin_amdgcn_s_setprio(1);
#pragma unroll 4
      for (int i = 0; i < 16; i++) glds((lane ^ i) * 16, (const char*)xin + (64 * m64 + 16 * (w - 4) + i) * 1024, smb + QKV_X + (16 * (w - 4) + i) * 1024);
      if (kvw) {
#pragma unroll
        for (int i = 0; i < 4; i++) glds4(l16, (const char*)((nt8 & 1) ? wv : wk) + i * 4096, smb + i * 4096);
        const unsigned to = lane < 16 ? WS_FKV + (g * 256 + r * 8 + (lane & 7)) * FL + (lane & 8) * (256 * FL * 2)
                                      : lane < 20 ? WS_KV + (lane & 3) * 0x10000 : min(WS_WOT + (lane - 20) * 0x200000, WS_GRP - 1024);
        glds(to, ws, smb + WARM_PAD);
      } else {
        __builtin_amdgcn_s_setprio(0);
        prep_load(min(vb * 3 + ((w - (nt8 >> 1) + 3) & 3), 735), lane, wg, wu, wd, wo, pv);
      }
    } else {
      __builtin_amdgcn_s_sleep(W_NAP);
      issue_wq(cx, (const char*)wq);
    }
    asm volatile("s_waitcnt lgkmcnt(0)" : "+s"(tag) :: "memory");
    tag = tag * 0x9E3779B97F4A7C15ull + 0x632BE59BD9B4E019ull;
    cx.tag = tag;
    phase_qkv<0>(cx, pv);
  }

#pragma unroll 1
  for (layer = 0; layer < 2; layer++) {
    const unsigned KB = WS_KV + layer * KV_SIZE;
    DnRegs R;
    auto attend = [&](auto ec) __attribute__((always_inline)) {
      constexpr bool EARLY = decltype(ec)::value;
      PHASE_IDS();
      const int hh = lane >> 5, row = lane & 31;
      unsigned ka = smb + (EARLY ? 0 : 4 * KVG) + l16;
      unsigned va = smb + (EARLY ? 0 : 4 * KVG) + ((row < 16) ? 1024 + hh * 256 + row * 16 : ((row & 11) == 0 ? 1536 : 1552));
      int idx = 0xdddddddd, qo = QKV_Q + (w & 3) * 2048 + l16;
      PIN(ka); PIN(va); PIN(idx); PIN(qo);
      const char* kvg = ws + KB;
      const unsigned dwo = WS_WDT + (nt8 * 8 + w) * 28672;
      const unsigned pa = w < 7 ? WS_WGUT + ((r & 15) * 7 + w) * 32768 : dwo + 10240;
      lds_bar();
      const u32x4 qa = lds_rd(sm, qo), qb = lds_rd(sm, qo + 1024);
      f32x16 Oa, Ob;
      float ma = 0.f, mb = 0.f;
      int ok;
      auto verdict = [&]() {
        const u32x4 okv = lds_rd(sm, S_OK & ~15), okw = lds_rd(sm, (S_OK & ~15) + 16);
        ok = __builtin_amdgcn_readfirstlane(okv[2] & okv[3] & okw[0] & okw[1]);
      };
      STAMP(7);
      if constexpr (!EARLY) {
        const int j = w - 4;
        __builtin_amdgcn_s_setprio(3);
        poll(ws + WS_FKV + (layer * 8 + g) * 256 * FL, (j * 64 + lane) * FL, tag);
        lds_inc(sm + S_KP);
        lds_wait<1>(sm + S_KP, 4 * layer + 4);
        if (layer == 0 && j == 3 && lane < 8) set_flag(ws, WS_FPREP + (lane * 256 + vb) * FL, tag);
        f32x2 kf;
        asm volatile("global_load_dwordx2 %0, %1, %2 sc1" : "=v"(kf) : "v"(lane * 8), "s"(ws + KB + KV_MAX) : "memory");
        auto piece = [&](int i) {
          const int pc = min(17 + 4 * i + j, 135);
          int src = pc + g * 17;
          if (src >= 136) src -= 136;
          glds(l16, kvg + src * 1024, smb + pc * 1024);
        };
#pragma unroll 5
        for (int i = 0; i < 15; i++) piece(i);
        asm volatile("s_waitcnt vmcnt(10)" : "+v"(kf) :: "memory");
        lds_inc(sm + S_KV);
#pragma unroll 5
        for (int i = 15; i < 30; i++) piece(i);
        lds_post(sm + S_OK + j * 4, att_bound_ok(qa, qb, fmaxf(kf[0], kf[1])));
        WAIT_VM(0);
        lds_inc(sm + S_KV + 4);
        if (layer == 0) {
          const char* tp; unsigned to;
          if (j == 0) { tp = ws + WS_FKV + (2048 + vb * 8) * FL; to = (lane & 7) * FL; }
          else if (j == 1) { tp = ws + gb + G_FO + r * 26 * FL; to = min(lane, 25) * FL; }
          else if (j == 2) { tp = ws + gb; to = lane * 0x7000; }
          else { tp = karg(A_OUT) + m64 * 65536; to = lane * 1024; }
          glds(to, tp, smb + GU_MF);
        }
        lds_wait<1>(sm + S_KV + 4, 4 * layer + 4);
        STAMP(8);
        verdict();
      }
      att_fast<EARLY>(ka, va, qa, qb, Oa, Ob, idx, smb + SY, 4 * layer + 4, ws + pa, ws + dwo, ws + WS_WOT + (nt8 * 64 + (w & 3) * 16) * 1024,
                      smb + ((w >> 1) & 1) * 4 * KVG + (w & 1) * 16384,
                      l16,
                      ws + WS_FPREP + (g * 256 + (w & 3) * 64) * FL, tag, R);
      STAMP(9);
      if constexpr (EARLY) verdict();
      if (!ok) {
        bar();
#pragma unroll 1
        for (int pc = w; pc < 102; pc += 8) { int src = pc + g * 17; if (src >= 136) src -= 136; glds(lane_id() * 16, kvg + src * 1024, smb + pc * 1024); }
        WAIT_VM(0);
        bar();
        ka -= smb + 2 * KVG; va -= smb + 2 * KVG;
        const int q2 = QKV_Q + (w & 3) * 2048 + lane_id() * 16;
        att_safe(sm, ka, va, lds_rd(sm, q2), Oa, ma, idx);
        att_safe(sm, ka, va, lds_rd(sm, q2 + 1024), Ob, mb, idx);
        lds_bar();
#pragma unroll 1
        for (int i = 0; i < 8; i++) glds(lane_id() * 16, ws + WS_WOT + (nt8 * 64 + w * 8 + i) * 1024, smb + (w >> 2) * 4 * KVG + ((w & 3) * 8 + i) * 1024);
        asm volatile("s_nop 7\n\ts_nop 7\n\ts_mov_b64 s[42:43], %[pa]\n\t" WLD8(84) WLD8(116) WLD8(148) WLD8(180)
                     "s_mov_b64 s[42:43], %[pb]\n\t" WLD4A(68) WLD4B(52) "s_waitcnt vmcnt(0)"
                     : "=&{v[68:83]}"(R.d0), "=&{v[52:67]}"(R.d1), "=&{v[84:115]}"(R.wp[0]), "=&{v[116:147]}"(R.wp[1]),
                       "=&{v[148:179]}"(R.wp[2]), "=&{v[180:211]}"(R.wp[3])
                     : [vo] "v"(lane_id() * 16), [pa] "s"(ws + pa + 4096), [pb] "s"(ws + dwo + 4096) : "s42", "s43", "scc", "memory");
      }
      att_finish<EARLY>(cx, layer, Oa, Ob, ma, mb);
    };
    if (w_ < 4) attend(BoolC<true>{}); else attend(BoolC<false>{});
    u32x4 cg, cu;
    R.dx = phase_wo(cx, layer, R, cg, cu);
    __builtin_amdgcn_s_setprio(2);
    gu_fetch(cx, layer);
    gu_pass<0>(cx, layer, R, cg, cu);
    __builtin_amdgcn_s_setprio(1);
    gu_pass<1>(cx, layer, R, cg, cu);
    __builtin_amdgcn_s_setprio(0);
    {
      PHASE_IDS();
      int wb = w * 8192 + l16, rb = (w >> 2) * 65536 + (w & 3) * 2048 + l16;
      unsigned oo = layer ? (m64 * 64 + (w >> 2) * 32 + (lane & 31)) * 1024 + nt8 * 128 + (w & 3) * 32 + (lane >> 5) * 16
                          : l16 + gb + G_X2 + ((m4 * 2 + (w >> 2)) * 32 + nt8 * 4 + (w & 3)) * 1024;
      const u32x4 dx = R.dx;
      u32x4 dw[28], hf[14];
      lds_wait<1>(sm + S_V, layer + 1);
#pragma unroll
      for (int i = 0; i < 14; i++) hf[i] = ld16(rs, l16, gb + G_H + (m4 * 2 * 112 + w * 14 + i) * 1024);
      float r2 = *(const LAS float*)(sm + GU_R2 + ((w >> 2) * 64 + ((r >> 3) & 1) * 32 + (lane & 31)) * 4);
      PIN(wb); PIN(rb); PIN(oo); PIN(r2);
      const char* po = karg(A_OUT);
      if (w == 7) h_flag(cx, layer, 1);
#pragma unroll
      for (int i = 0; i < 28; i++) dw[i] = i < 4 ? frag(R.d0, i & 3) : i < 8 ? frag(R.d1, i & 3) : i < 10 ? frag(R.d2, i & 1) : frag(R.wp[((i + 22) & 31) >> 3], (i + 22) & 7);
#pragma unroll
      for (int ps = 0; ps < 2; ps++) {
        f32x16 acc[2] = {};
#pragma unroll
        for (int i = 0; i < 14; i++) {
          SCHED();
          acc[0] = mfma32(dw[2 * i], hf[i], acc[0]);
          acc[1] = mfma32(dw[2 * i + 1], hf[i], acc[1]);
          SCHED();
        }
        if (ps) wb += 65536;
#pragma unroll
        for (int n = 0; n < 2; n++)
#pragma unroll
          for (int q = 0; q < 4; q++)
            *(LAS f32x4*)(sm + wb + (n * 4 + q) * 1024) = f32x4{acc[n][4 * q], acc[n][4 * q + 1], acc[n][4 * q + 2], acc[n][4 * q + 3]};
        if (ps) lds_bar();
        else {
          STAMP(24);
          if (layer == 0 && w < 4) issue_wkv(cx, karg(A_WK + (nt8 & 1)));
          if (w == 0) { lds_wait<1>(sm + S_H + 4, 7 * layer + 7); __builtin_amdgcn_s_sleep(B_NAP); h_poll(cx, layer, 1); }
          else lds_wait<1>(sm + S_V + 4, layer + 1);
          STAMP(23);
#pragma unroll
          for (int i = 0; i < 14; i++) hf[i] = ld16(rs, l16, gb + G_H + ((m4 * 2 + 1) * 112 + w * 14 + i) * 1024);
        }
        if (ps) {
          f32x4 s0 = lds_rdf(sm, rb), s1 = lds_rdf(sm, rb + 1024);
#pragma unroll
          for (int i = 1; i < 8; i++) { s0 += lds_rdf(sm, rb + i * 8192); s1 += lds_rdf(sm, rb + i * 8192 + 1024); }
          s0 = s0 * r2 + f32x4{bf_lo(dx[0]), bf_hi(dx[0]), bf_lo(dx[1]), bf_hi(dx[1])};
          s1 = s1 * r2 + f32x4{bf_lo(dx[2]), bf_hi(dx[2]), bf_lo(dx[3]), bf_hi(dx[3])};
          const u32x4 o = {pack2(s0[0], s0[1]), pack2(s0[2], s0[3]), pack2(s1[0], s1[1]), pack2(s1[2], s1[3])};
          if (layer == 0) st16(rs, oo, 0, o);
          else __builtin_amdgcn_raw_buffer_store_b128(o, __builtin_amdgcn_make_buffer_rsrc((void*)po, 0, 2048 * 1024, 0x00020000), oo, 0, 0);
        }
        STAMP(19 + ps);
      }
      if (layer == 0) {
        WAIT_VM(0);
        if (BACK & 8) glds(oo, ws, smb + WARM_PAD);
        STAMP(22);
        u32x4 none[8];
        phase_qkv<1>(cx, none);
      }
    }
  }
  if (blockIdx.x == 0 && w_ == 0 && lane_id() == 0) set_flag(ws, WS_EPOCH, tag);
}

extern "C" void kernel_launch(void* const* d_in, const int* in_sizes, int n_in, void* d_out, int out_size, void* d_ws,
                              size_t ws_size, hipStream_t stream) {
  (void)hipFuncSetAttribute((const void*)k_mega, hipFuncAttributeMaxDynamicSharedMemorySize, LDS_BYTES);
  k_mega<<<256, 512, LDS_BYTES, stream>>>((const u16*)d_in[0], (const u16*)d_in[1], (const u16*)d_in[2], (const u16*)d_in[3],
                                          (const u16*)d_in[4], (const u16*)d_in[6], (const u16*)d_in[7], (const u16*)d_in[8],
                                          (char*)d_ws, (u16*)d_out);
}
```
